```python
import torch

def _softmax(x, axis=-1):
    return torch.softmax(x, dim=axis)
def _gelu(x):
    return torch.nn.functional.gelu(x, approximate="tanh")
def _silu(x):
    return torch.nn.functional.silu(x)
def _sigmoid(x):
    return torch.sigmoid(x)
def _layernorm(x, axis=-1, eps=1e-5):
    m = x.mean(dim=axis, keepdim=True)
    v = x.var(dim=axis, keepdim=True, unbiased=False)
    return (x - m) / torch.sqrt(v + eps)
def _scatter_add(tgt, idx, src, axis=0):
    return tgt.clone().index_add_(axis, idx, src)
def _segment_sum(src, seg, n):
    out = torch.zeros((n,) + src.shape[1:], dtype=src.dtype)
    return out.index_add_(0, seg, src)
def _pad(x, pw):
    flat = tuple(v for pair in reversed(pw) for v in pair)
    return torch.nn.functional.pad(x, flat)

kernel_name = 'standalone__kv_cache_quant_int4_int2_kv_with_per_channel_or_per_token_scales__decode'

def setup_inputs():

    _w = (lambda a: (lambda t: t.to(torch.bfloat16) if t.is_floating_point() else t)(torch.as_tensor(a)))
    B = 4
    Sq = 1
    Skv = 8192
    Hq = 32
    Hkv = 8
    Dh = 128
    bits = 2
    G = 64
    if G <= 0 or G >= Skv:
        G = max(1, Skv // 2)
    Skv = (Skv // G) * G
    ngk = Skv // G
    qmax = (1 << bits) - 1
    return {
        "q": _w((lambda *s: torch.randn(*s, dtype=torch.bfloat16) * 3.2)(B, Hq, Sq, Dh)),
        "qk": _w(torch.randint(0, qmax + 1, (B, Hkv, Skv, Dh)).to(torch.int32)),
        "qv": _w(torch.randint(0, qmax + 1, (B, Hkv, Skv, Dh)).to(torch.int32)),
        "k_scales": _w(torch.abs((lambda *s: torch.randn(*s, dtype=torch.bfloat16) * 3.2)(B, Hkv, ngk, Dh)) * 0.05 + 0.02),
        "k_zeros": _w((lambda *s: torch.randn(*s, dtype=torch.bfloat16) * 3.2)(B, Hkv, ngk, Dh) + float(qmax) / 2.0),
        "v_scales": _w(torch.abs((lambda *s: torch.randn(*s, dtype=torch.bfloat16) * 0.05)(B, Hkv, Skv, 1)) * 0.05 + 0.02),
        "v_zeros": _w((lambda *s: torch.randn(*s, dtype=torch.bfloat16) * 0.05)(B, Hkv, Skv, 1) + float(qmax) / 2.0),
    }

def reference(q, qk, qv, k_scales, k_zeros, v_scales, v_zeros):
    B, Hq, Sq, Dh = q.shape
    Hkv, Skv = qk.shape[1], qk.shape[2]
    ngk = k_scales.shape[2]
    G = Skv // ngk
    ks = (lambda x, n, axis: torch.repeat_interleave(x, n, dim=axis))(k_scales, G, axis=2)
    kz = (lambda x, n, axis: torch.repeat_interleave(x, n, dim=axis))(k_zeros, G, axis=2)
    k = ((lambda x, dt: x.to(dt))(qk, q.dtype) - kz) * ks
    v = ((lambda x, dt: x.to(dt))(qv, q.dtype) - v_zeros) * v_scales
    rep = Hq // Hkv
    if rep > 1:
        k = (lambda x, n, axis: torch.repeat_interleave(x, n, dim=axis))(k, rep, axis=1)
        v = (lambda x, n, axis: torch.repeat_interleave(x, n, dim=axis))(v, rep, axis=1)
    scale = float(Dh) ** -0.5
    logits = torch.einsum("bhqd,bhkd->bhqk", q, k) * scale
    p = _softmax(logits, axis=-1)
    o = torch.einsum("bhqk,bhkd->bhqd", p, v)
    return o
```

```cpp
#include <hip/hip_runtime.h>
#include <hip/hip_bf16.h>
#include <stdint.h>

namespace {
constexpr int NBATCH = 4, HQ = 32, HKV = 8, SKV = 8192, DH = 128;
constexpr int NBH = NBATCH * HKV;
constexpr int NSPLIT = 8;
constexpr int NCW = 4;
constexpr int NT = 16;
constexpr int NLT = 2 * NT - 1;
constexpr int NTHR = (NCW + 1) * 64;
constexpr int KV_BYTES = NBH * SKV * DH * 4;
constexpr int NFL = 22;

constexpr int SB_[9] = {13, 14, 11, 12, 15, 16, 17, 10, 19};
constexpr int BB_[8] = {23, 22, 25, 18, 20, 21, 24, 26};
__host__ __device__ constexpr int pos2off(int x) { int o = 0; for (int k = 0; k < 9; ++k) o |= ((x >> k) & 1) << SB_[k]; return o; }
__host__ __device__ constexpr int pos2g(int x) { return ((pos2off(x) >> 15) & 7) | ((pos2off(x) >> 16) & 8); }
__host__ __device__ constexpr int pos2vp(int x) { return pos2g(x) * 128 + ((pos2off(x) >> 8) & 0x7c); }
static_assert(pos2off(511) == 0xbfc00, "a workgroup covers 16 whole groups");
static_assert(pos2g(15) == 0 && pos2g(16) == 1, "rows 0-3 and 4-7 of a tile lie in two neighbouring groups");

typedef unsigned u32x4 __attribute__((ext_vector_type(4)));
typedef unsigned u32x2 __attribute__((ext_vector_type(2)));
typedef int i32x4 __attribute__((ext_vector_type(4)));
typedef float f32x4 __attribute__((ext_vector_type(4)));
typedef float f32x2 __attribute__((ext_vector_type(2)));
typedef __bf16 bf16x2 __attribute__((ext_vector_type(2)));
typedef __bf16 bf16x8 __attribute__((ext_vector_type(8)));
template <bool B> struct BoolC { static constexpr bool value = B; };
#define LAS __attribute__((address_space(3)))

__device__ __forceinline__ unsigned pk(float lo, float hi) {
  bf16x2 v = {(__bf16)lo, (__bf16)hi};
  return __builtin_bit_cast(unsigned, v);
}
__device__ __forceinline__ float lo16(unsigned u) { return __builtin_bit_cast(float, u << 16); }
__device__ __forceinline__ float hi16(unsigned u) { return __builtin_bit_cast(float, u & 0xffff0000u); }
__device__ __forceinline__ float rbf(float x) { return (float)(__bf16)x; }
__device__ __forceinline__ f32x4 mfma(u32x4 a, u32x4 b, f32x4 c) {
  return __builtin_amdgcn_mfma_f32_16x16x32_bf16(__builtin_bit_cast(bf16x8, a), __builtin_bit_cast(bf16x8, b), c, 0, 0, 0);
}
template <int CTRL> __device__ __forceinline__ float dpp(float x) {
  return __builtin_bit_cast(float, __builtin_amdgcn_mov_dpp(__builtin_bit_cast(int, x), CTRL, 0xf, 0xf, true));
}
__device__ __forceinline__ float wave_sum(float x) {
  x += dpp<0x128>(x); x += dpp<0x124>(x); x += dpp<0x122>(x); x += dpp<0x121>(x);
  auto s = __builtin_amdgcn_permlane16_swap(__float_as_uint(x), __float_as_uint(x), false, false);
  x = __uint_as_float(s[0]) + __uint_as_float(s[1]);
  auto t = __builtin_amdgcn_permlane32_swap(__float_as_uint(x), __float_as_uint(x), false, false);
  return __uint_as_float(t[0]) + __uint_as_float(t[1]);
}
#define WAVE_FENCE() do { __builtin_amdgcn_fence(__ATOMIC_ACQ_REL, "wavefront"); __builtin_amdgcn_wave_barrier(); } while (0)
#define SB() __builtin_amdgcn_sched_barrier(0)
#define LDS_WAIT() asm volatile("s_waitcnt lgkmcnt(0)" ::: "memory")
#define LDS_BARRIER() asm volatile("s_waitcnt lgkmcnt(0)\n\ts_barrier" ::: "memory")

constexpr int SLOT = 32 * 1024, RING = 3 * SLOT;
constexpr int KZ_OFF = RING, KS_OFF = KZ_OFF + 4096;
constexpr int VS_OFF = KS_OFF + 4096;
constexpr int VZ_OFF = VS_OFF + 2048;
constexpr int Q_OFF = VZ_OFF + 2048;
constexpr int PRIV = Q_OFF + 1024;
constexpr int KT_RS = 144, KT_HALF = 16 * KT_RS, KT_BYTES = 2 * KT_HALF;
constexpr int PT_OFF = KT_BYTES;
constexpr int SC_OFF = PT_OFF + 1024;
constexpr int PRIV_W = SC_OFF + 4096;
constexpr int CTL = PRIV + NCW * PRIV_W;
constexpr int C_FREE = CTL, C_FULL = CTL + 32, C_MX = CTL + 48, C_LC = C_MX + NCW * 16;
constexpr int LDS_BYTES = C_LC + NCW * 16;
static_assert(LDS_BYTES <= 160 * 1024, "LDS");

template <int J> __device__ __forceinline__ void drain_rows(unsigned fa) {
  asm volatile("s_waitcnt vmcnt(%0)\n\tds_write_b32 %1, %2" :: "i"(4 * J), "v"(fa), "v"(NLT * 32 - 4 * J) : "memory");
  if constexpr (J > 0) drain_rows<J - 1>(fa);
}

__global__ void __launch_bounds__(NTHR, 2) __attribute__((aligned(8192)))
kv_split(const int* __restrict__ qk, const int* __restrict__ qv,
         const unsigned short* __restrict__ kzr, const unsigned short* __restrict__ ksc,
         const unsigned short* __restrict__ q,
         const unsigned short* __restrict__ vsc, const unsigned short* __restrict__ vzr,
         float* __restrict__ part) {
  __shared__ __attribute__((aligned(16))) unsigned char smem[LDS_BYTES];
  const int tid = threadIdx.x, lane = tid & 63;
  const int wv = __builtin_amdgcn_readfirstlane(tid >> 6);
  const int bid = blockIdx.x;
  int gbase = 0;
#pragma unroll
  for (int k = 0; k < 8; ++k) {
    if (k > 0 && BB_[k] == BB_[k - 1] + 1) continue;
    const int n = (k < 7 && BB_[k + 1] == BB_[k] + 1) ? 3 : 1;
    gbase |= ((bid >> k) & n) << BB_[k];
  }
  const int voff = lane * 16;
  asm volatile("" : "+s"(gbase) : "s"(qk), "s"(qv), "s"(kzr), "s"(ksc), "s"(q), "s"(vsc), "s"(vzr), "s"(part));
  if (wv == 0) {
    __builtin_amdgcn_s_setprio(3);
    const unsigned lb = (unsigned)(size_t)(LAS unsigned char*)smem;
    const unsigned fa = lb + C_FULL, fr = lb + C_FREE;
    const int k7 = voff + ((unsigned)gbase >> 7), k8 = voff + ((unsigned)gbase >> 8);
#define PAR2(M0, VA, P, O0, O1) asm volatile("s_mov_b32 m0, %0\n\ts_nop 0\n\tglobal_load_lds_dwordx4 %1, %2 offset:" #O0 " nt\n\tglobal_load_lds_dwordx4 %1, %2 offset:" #O1 " nt" \
                                          :: "s"(lb + (M0)), "v"(VA), "s"(P) : "memory")
#define PAR1(M0, VA, P, POL) asm volatile("s_mov_b32 m0, %0\n\ts_nop 0\n\tglobal_load_lds_dwordx4 %1, %2" POL :: "s"(lb + (M0)), "v"(VA), "s"(P) : "memory")
    PAR2(KZ_OFF, k7, kzr, 0, 1024); PAR2(KS_OFF, k7, ksc, 0, 1024);
    PAR1(Q_OFF, voff + (gbase >> 22) * 1024, q, "");
    PAR2(KZ_OFF, k7 + 2048, kzr, 2048, 3072); PAR2(KS_OFF, k7 + 2048, ksc, 2048, 3072);
    PAR1(VS_OFF, k8, vsc, " nt"); PAR1(VZ_OFF, k8, vzr, " nt"); PAR1(VS_OFF + 1024, k8 + 2048, vsc, " nt"); PAR1(VZ_OFF + 1024, k8 + 2048, vzr, " nt");
    static_assert(pos2off(127) == 0x3f800 && SB_[2] < SB_[3] && SB_[3] < SB_[4] && SB_[4] < SB_[5] && SB_[5] < SB_[6], "address walk of the loader");
    const int vo1 = voff + pos2off(1) - 1024, vo2 = voff + pos2off(2) - 2048, vo3 = voff + pos2off(3) - 3072;
    unsigned mb = lb;
    int so = gbase, it = 0;
    int cnt = (4 - NFL) & ~3;
    unsigned long long src = (unsigned long long)qk;
    i32x4 f;
    asm volatile("" : "=v"(f));
#define ROW(J, V) "s_waitcnt vmcnt(%9)\n\tbuffer_load_dwordx4 " V ", %6, %7 offen offset:" #J "*1024 nt lds\n\t"
#pragma unroll 1
    do {
      const __amdgpu_buffer_rsrc_t rs = __builtin_amdgcn_make_buffer_rsrc((void*)src, 0, KV_BYTES, 0x00020000);
      asm volatile("s_mov_b32 m0, %1\n\t" ROW(0, "%2") ROW(1, "%3") ROW(2, "%4") ROW(3, "%5") "ds_write_b32 %8, %0\n\tv_add_u32 %0, 4, %0"
                   : "+v"(cnt) : "s"(mb), "v"(voff), "v"(vo1), "v"(vo2), "v"(vo3), "s"(rs), "s"(so), "v"(fa), "i"(NFL - 1) : "memory");
      ++it;
      mb += 4096;
      so = ((so | pos2off(3)) + pos2off(4)) & ~pos2off(3);
      if ((it & 7) == 7) asm volatile("ds_read_b128 %0, %1" : "=v"(f) : "v"(fr) : "memory");
      if ((it & 7) == 0) {
        const int T = it >> 3;
        if (T == 1) LDS_BARRIER();
        if (mb == lb + RING) mb = lb;
        if ((T & 3) == 0) { so = gbase | pos2off((T & 12) << 5); if (T == NT) src = (unsigned long long)qv; }
        asm volatile("s_waitcnt lgkmcnt(0)" : "+v"(f) :: "memory");
        while (T > 2 && T < NLT && __builtin_amdgcn_readfirstlane(min(min(f[0], f[1]), min(f[2], f[3]))) < T - 2) {
          __builtin_amdgcn_s_sleep(1);
          asm volatile("ds_read_b128 %0, %1\n\ts_waitcnt lgkmcnt(0)" : "=v"(f) : "v"(fr) : "memory");
        }
      }
    } while (it < NLT * 8);
    drain_rows<(NFL - 1) / 4>(fa);
    asm volatile("s_waitcnt lgkmcnt(0)\n\ts_endpgm");
  }
  const int w = wv - 1;
  const int bh = gbase >> 22;
  const int lg = lane >> 4, c = lane & 15, r = lane & 31, h = lane >> 5;
  volatile LAS int* ctl = (volatile LAS int*)(smem + CTL);
  if (lane == 0) { ctl[w] = 0; ctl[4 + w] = 0; }
#define POST_STAGE(v) do { LDS_WAIT(); if (lane == 0) ctl[4 + w] = (v); } while (0)
#define WAIT_STAGE(v) do { while ((__builtin_amdgcn_readfirstlane(ctl[4] & ctl[5] & ctl[6] & ctl[7]) & (v)) != (v)) __builtin_amdgcn_s_sleep(1); asm volatile("" ::: "memory"); } while (0)
#define WAIT_ROWS(n) do { while (__builtin_amdgcn_readfirstlane(ctl[8]) < (n)) __builtin_amdgcn_s_sleep(1); asm volatile("" ::: "memory"); } while (0)
  LDS_BARRIER();
  unsigned char* kt = smem + PRIV + w * PRIV_W;
  const unsigned char* ringw = smem + w * 1024 + voff;
  unsigned char* kw = kt + (r >> 4) * KT_HALF + h * KT_RS + (r & 15) * 8;
  const unsigned char* kr = kt + (lg & 1) * KT_HALF + c * KT_RS + (lg >> 1) * 16;
  WAIT_ROWS(4);
  u32x4 Q[4];
#pragma unroll
  for (int s = 0; s < 4; ++s) Q[s] = *(const u32x4*)(smem + Q_OFF + (c >> 2) * (DH * 2) + 128 * (lg & 1) + 16 * (lg >> 1) + 32 * s);
  constexpr float SCALE = 0.088388347648318440f, LOG2E = 1.4426950408889634f;
  float mx = -INFINITY;
  int so = 0;

#pragma unroll 1
  for (int T = 0; T < NT; ++T) {
    const unsigned char* kp = smem + KZ_OFF + pos2g(T << 5) * 256 + r * 8;
    u32x2 kzp[2], ksp[2];
#pragma unroll
    for (int hf = 0; hf < 2; ++hf) { kzp[hf] = *(const u32x2*)(kp + hf * 256); ksp[hf] = *(const u32x2*)(kp + hf * 256 + 4096); }
    WAIT_ROWS(T * 32 + 32);
    u32x4 x[8];
#pragma unroll
    for (int i = 0; i < 8; ++i) x[i] = *(const u32x4*)(ringw + so + i * 4096);
    LDS_WAIT();
    if (lane == 0) ctl[w] = T + 1;
    asm volatile("" : "+v"(x[0]), "+v"(x[1]), "+v"(x[2]), "+v"(x[3]), "+v"(x[4]), "+v"(x[5]), "+v"(x[6]), "+v"(x[7]) :: "memory");
    so = so == 2 * SLOT ? 0 : so + SLOT;
#pragma unroll
    for (int i = 0; i < 8; ++i) {
      const int hf = i >> 2;
      const float kz0 = lo16(kzp[hf][0]), kz1 = hi16(kzp[hf][0]), kz2 = lo16(kzp[hf][1]), kz3 = hi16(kzp[hf][1]);
      const float ks0 = lo16(ksp[hf][0]), ks1 = hi16(ksp[hf][0]), ks2 = lo16(ksp[hf][1]), ks3 = hi16(ksp[hf][1]);
      const unsigned t01 = pk((float)(int)x[i][0] - kz0, (float)(int)x[i][1] - kz1), t23 = pk((float)(int)x[i][2] - kz2, (float)(int)x[i][3] - kz3);
      u32x2 kk;
      kk[0] = pk(lo16(t01) * ks0, hi16(t01) * ks1);
      kk[1] = pk(lo16(t23) * ks2, hi16(t23) * ks3);
      *(u32x2*)(kw + i * 2 * KT_RS) = kk;
    }
    WAVE_FENCE();
    f32x4 d = {0.f, 0.f, 0.f, 0.f};
#pragma unroll
    for (int s = 0; s < 4; ++s) {
      const u32x4 bf = *(const u32x4*)(kr + s * 32);
      d = mfma(Q[s], bf, d);
    }
    WAVE_FENCE();
    const float sc = rbf(rbf(d[0]) * SCALE);
    mx = fmaxf(mx, sc);
    *(float*)(kt + SC_OFF + T * 256 + c * 16 + lg * 4) = sc;
  }
  mx = fmaxf(mx, dpp<0x128>(mx)); mx = fmaxf(mx, dpp<0x124>(mx)); mx = fmaxf(mx, dpp<0x122>(mx)); mx = fmaxf(mx, dpp<0x121>(mx));
  if (c == 0) *(float*)(smem + C_MX + w * 16 + lg * 4) = mx;
  POST_STAGE(1);
  WAIT_STAGE(1);
  f32x4 m = *(const f32x4*)(smem + C_MX);
#pragma unroll
  for (int ww = 1; ww < NCW; ++ww) {
    const f32x4 o = *(const f32x4*)(smem + C_MX + ww * 16);
#pragma unroll
    for (int j = 0; j < 4; ++j) m[j] = fmaxf(m[j], o[j]);
  }
  float lsum[4], cz[4];
  f32x4 acc[4];
#pragma unroll
  for (int j = 0; j < 4; ++j) { lsum[j] = 0.f; cz[j] = 0.f; acc[j] = f32x4{0.f, 0.f, 0.f, 0.f}; }
  const bool live = ((lane >> 3) & 1) == ((lane >> 4) & 1);
  const int lanev = pos2vp((lg << 5) | ((c >> 1) << 2) | w) + (c & 1) * 2;
  unsigned char* pt = kt + PT_OFF;
  int split = 0;
  {
    int s = 0;
#pragma unroll
    for (int k = 0; k < 8; ++k) if (BB_[k] < 22) { split |= ((bid >> k) & 1) << s; ++s; }
  }
  const unsigned fulla = (unsigned)(size_t)(LAS unsigned char*)smem + C_FULL, ringa = (unsigned)(size_t)(LAS unsigned char*)smem + w * 1024 + voff;
  const __amdgpu_buffer_rsrc_t rsV = __builtin_amdgcn_make_buffer_rsrc((void*)qv, 0, KV_BYTES, 0x00020000);
  const int lastb = gbase | pos2off((NT - 1) << 5);
  const int lastv = pos2off(lg) + w * 128 + c * 8;
  u32x2 dr[16];
  auto v_tile = [&](const int T, auto peel_) {
    constexpr bool PEEL = decltype(peel_)::value;
    u32x4 A = *(const u32x4*)(pt + (T & 3) * 256 + h * 128 + (lane & 7) * 16);
    if (!live) A = u32x4{0u, 0u, 0u, 0u};
    u32x4 Bf[4];
#pragma unroll
    for (int mm = 0; mm < 4; ++mm) {
      u32x4 xa, xb;
      while (true) {
        int fv;
        asm volatile("ds_read_b32 %0, %3\n\tds_read_b128 %1, %4 offset:%5\n\tds_read_b128 %2, %4 offset:%6\n\ts_waitcnt lgkmcnt(0)"
                     : "=&v"(fv), "=&v"(xa), "=&v"(xb) : "v"(fulla), "v"(ringa + so), "i"(mm * 8192), "i"(mm * 8192 + 4096) : "memory");
        if (__builtin_amdgcn_readfirstlane(fv) >= (NT + T) * 32 + mm * 8 + 8) break;
        if (!PEEL) __builtin_amdgcn_s_sleep(2);
      }
      if (PEEL && (mm & 1)) {
#pragma unroll
        for (int i = 4 * (mm - 1); i < 4 * (mm + 1); ++i) { dr[i] = __builtin_amdgcn_raw_buffer_load_b64(rsV, lastv + (i & 1) * 512, lastb + pos2off((i >> 1) << 2), 2); SB(); }
      }
      if (mm == 3 && lane == 0) ctl[w] = NT + T + 1;
#pragma unroll
      for (int e = 0; e < 4; ++e) { unsigned t = pk((float)(int)xa[e], (float)(int)xb[e]); asm volatile("" : "+v"(t)); Bf[e][mm] = t; }
    }
    so = so == 2 * SLOT ? 0 : so + SLOT;
#pragma unroll
    for (int e = 0; e < 4; ++e) acc[e] = mfma(A, Bf[e], acc[e]);
    WAVE_FENCE();
  };
#pragma unroll 1
  for (int T = 0; T < NT - 2; ++T) {
    if ((T & 3) == 0) {
      const int tk2 = lanev ^ pos2vp(T << 5);
      const f32x4 S = *(const f32x4*)(kt + SC_OFF + T * 256 + voff);
      const float vs = lo16(*(const unsigned short*)(smem + VS_OFF + tk2));
      const float vz = lo16(*(const unsigned short*)(smem + VZ_OFF + tk2));
      unsigned char* pw = pt + lg * 256 + (c & 1) * 128 + (c >> 1) * 2;
#pragma unroll
      for (int j = 0; j < 4; ++j) {
        const float p = __builtin_amdgcn_exp2f((S[j] - m[j]) * LOG2E);
        const float pv = p * vs;
        const float hi = rbf(pv), lo = rbf(pv - hi);
        lsum[j] += p;
        cz[j] += (hi + lo) * vz;
        const int rt = (j & 1) + 4 * (j >> 1);
        *(unsigned short*)(pw + rt * 16) = (unsigned short)(__builtin_bit_cast(unsigned, hi) >> 16);
        *(unsigned short*)(pw + (rt + 2) * 16) = (unsigned short)(__builtin_bit_cast(unsigned, lo) >> 16);
      }
      if (T == NT - 4) {
        f32x4 lv, cv;
#pragma unroll
        for (int j = 0; j < 4; ++j) { lv[j] = wave_sum(lsum[j]); cv[j] = wave_sum(cz[j]); }
        const float c0 = (lg & 1) ? cv[2] : cv[0], c1 = (lg & 1) ? cv[3] : cv[1];
#pragma unroll
        for (int e = 0; e < 4; ++e) { acc[e][0] -= c0; acc[e][1] -= c1; }
        if (lane == 0) *(f32x4*)(smem + C_LC + w * 16) = lv;
        POST_STAGE(3);
      }
      WAVE_FENCE();
    }
    v_tile(T, BoolC<false>{});
    if (T == NT - 3 && w == 0) {
      WAIT_STAGE(3);
      if (lane < 4) {
        float L = 0.f, M = -INFINITY;
#pragma unroll
        for (int ww = 0; ww < NCW; ++ww) { L += *(const float*)(smem + C_LC + ww * 16 + lane * 4); M = fmaxf(M, *(const float*)(smem + C_MX + ww * 16 + lane * 4)); }
        *(f32x2*)(part + ((bh * 4 + lane) * NSPLIT + split) * 2) = f32x2{M, L};
      }
    }
  }
  v_tile(NT - 2, BoolC<true>{});
  SB();
  {
    f32x4 o0, o1;
#pragma unroll
    for (int e = 0; e < 4; ++e) { o0[e] = acc[e][0] + acc[e][2]; o1[e] = acc[e][1] + acc[e][3]; }
    float* ow = (float*)kt + 2 * (lg & 1) * DH + 64 * (lg >> 1) + 4 * c;
    *(f32x4*)ow = o0;
    *(f32x4*)(ow + DH) = o1;
  }
  LDS_BARRIER();
  f32x2 O = {0.f, 0.f};
#pragma unroll
  for (int ww = 0; ww < NCW; ++ww) O += *(const f32x2*)(smem + PRIV + ww * PRIV_W + (lg * DH + 32 * w + 2 * c) * 4);
  const unsigned char* pa = smem + PRIV + lg * PRIV_W + PT_OFF + 3 * 256 + (((c >> 2) & 1) + 2 * (c & 1) + 4 * (c >> 3)) * 16;
  const u32x4 A0 = *(const u32x4*)pa, A1 = *(const u32x4*)(pa + 128);
  f32x4 d0 = {O[0], 0.f, 0.f, 0.f}, d1 = {O[1], 0.f, 0.f, 0.f};
  unsigned oo = (4096 + ((bh * 4 + lg) * NSPLIT + split) * DH + 32 * w + 2 * c) * 4;
  asm volatile("" : "+v"(d0), "+v"(d1), "+v"(oo));
  SB();
  u32x4 B[2][2];
#pragma unroll
  for (int mm = 0; mm < 4; ++mm) {
#pragma unroll
    for (int ks = 0; ks < 2; ++ks)
#pragma unroll
      for (int e = 0; e < 2; ++e) B[ks][e][mm] = pk((float)(int)dr[4 * mm + ks][e], (float)(int)dr[4 * mm + 2 + ks][e]);
    SB();
  }
  d0 = mfma(A0, B[0][0], d0); d1 = mfma(A0, B[0][1], d1);
  d0 = mfma(A1, B[1][0], d0); d1 = mfma(A1, B[1][1], d1);
  *(f32x2*)((char*)part + oo) = f32x2{d0[0] + d0[1], d1[0] + d1[1]};
}

__global__ void __launch_bounds__(64)
kv_combine(const float* __restrict__ part, unsigned short* __restrict__ out) {
  int bh = 0, rest = blockIdx.x >> 3;
#pragma unroll
  for (int k = 0; k < 3; ++k) bh |= ((blockIdx.x >> k) & 1) << (BB_[k] - 22);
#pragma unroll
  for (int k = 3; k < 8; ++k) if (BB_[k] >= 22) { bh |= (rest & 1) << (BB_[k] - 22); rest >>= 1; }
  const int row = bh * 4 + rest, d = threadIdx.x * 2;
  float mv[NSPLIT], lv[NSPLIT];
  f32x2 ov[NSPLIT];
#pragma unroll
  for (int s = 0; s < NSPLIT; ++s) ov[s] = *(const f32x2*)(part + 4096 + (row * NSPLIT + s) * DH + d);
#pragma unroll
  for (int s = 0; s < NSPLIT; ++s) { mv[s] = part[(row * NSPLIT + s) * 2]; lv[s] = part[(row * NSPLIT + s) * 2 + 1]; }
  float M = -INFINITY;
#pragma unroll
  for (int s = 0; s < NSPLIT; ++s) M = fmaxf(M, mv[s]);
  float L = 0.f, wt[NSPLIT];
#pragma unroll
  for (int s = 0; s < NSPLIT; ++s) { wt[s] = __builtin_amdgcn_exp2f((mv[s] - M) * 1.4426950408889634f); L += wt[s] * lv[s]; }
  const float inv = 1.0f / L;
  f32x2 O = ov[0] * wt[0];
#pragma unroll
  for (int s = 1; s < NSPLIT; ++s) O += ov[s] * wt[s];
  *(unsigned*)(out + row * DH + d) = pk(O[0] * inv, O[1] * inv);
}
}

extern "C" void kernel_launch(void* const* d_in, const int* in_sizes, int n_in, void* d_out, int out_size,
                              void* d_ws, size_t ws_size, hipStream_t stream) {
  const unsigned short* q = (const unsigned short*)d_in[0];
  const int* qk = (const int*)d_in[1];
  const int* qv = (const int*)d_in[2];
  const unsigned short* ks = (const unsigned short*)d_in[3];
  const unsigned short* kz = (const unsigned short*)d_in[4];
  const unsigned short* vs = (const unsigned short*)d_in[5];
  const unsigned short* vz = (const unsigned short*)d_in[6];
  float* part = (float*)d_ws;
  kv_split<<<NBH * NSPLIT, NTHR, 0, stream>>>(qk, qv, kz, ks, q, vs, vz, part);
  kv_combine<<<NBATCH * HQ, 64, 0, stream>>>(part, (unsigned short*)d_out);
}
```

```cpp
#include <hip/hip_runtime.h>
#include <hip/hip_bf16.h>
#include <stdint.h>

#define RCP 1
namespace {
constexpr int NBATCH = 4, HQ = 32, HKV = 8, SKV = 8192, DH = 128;
constexpr int NBH = NBATCH * HKV;
constexpr int NSPLIT = 8;
constexpr int NCW = 4;
constexpr int NT = 16;
constexpr int NLT = 2 * NT - 1;
constexpr int NTHR = (NCW + 1) * 64;
constexpr int KV_BYTES = NBH * SKV * DH * 4;
constexpr int NFL = 22;

constexpr int SB_[9] = {13, 14, 11, 12, 15, 16, 17, 10, 19};
constexpr int BB_[8] = {23, 22, 25, 18, 20, 21, 24, 26};
__host__ __device__ constexpr int pos2off(int x) { int o = 0; for (int k = 0; k < 9; ++k) o |= ((x >> k) & 1) << SB_[k]; return o; }
__host__ __device__ constexpr int pos2g(int x) { return ((pos2off(x) >> 15) & 7) | ((pos2off(x) >> 16) & 8); }
__host__ __device__ constexpr int pos2vp(int x) { return pos2g(x) * 128 + ((pos2off(x) >> 8) & 0x7c); }
static_assert(pos2off(511) == 0xbfc00, "a workgroup covers 16 whole groups");
static_assert(pos2g(15) == 0 && pos2g(16) == 1, "rows 0-3 and 4-7 of a tile lie in two neighbouring groups");

typedef unsigned u32x4 __attribute__((ext_vector_type(4)));
typedef unsigned u32x2 __attribute__((ext_vector_type(2)));
typedef int i32x4 __attribute__((ext_vector_type(4)));
typedef float f32x4 __attribute__((ext_vector_type(4)));
typedef float f32x2 __attribute__((ext_vector_type(2)));
typedef __bf16 bf16x2 __attribute__((ext_vector_type(2)));
typedef __bf16 bf16x8 __attribute__((ext_vector_type(8)));
template <bool B> struct BoolC { static constexpr bool value = B; };
#define LAS __attribute__((address_space(3)))

__device__ __forceinline__ unsigned pk(float lo, float hi) {
  bf16x2 v = {(__bf16)lo, (__bf16)hi};
  return __builtin_bit_cast(unsigned, v);
}
__device__ __forceinline__ float lo16(unsigned u) { return __builtin_bit_cast(float, u << 16); }
__device__ __forceinline__ float hi16(unsigned u) { return __builtin_bit_cast(float, u & 0xffff0000u); }
__device__ __forceinline__ float rbf(float x) { return (float)(__bf16)x; }
__device__ __forceinline__ f32x4 mfma(u32x4 a, u32x4 b, f32x4 c) {
  return __builtin_amdgcn_mfma_f32_16x16x32_bf16(__builtin_bit_cast(bf16x8, a), __builtin_bit_cast(bf16x8, b), c, 0, 0, 0);
}
template <int CTRL> __device__ __forceinline__ float dpp(float x) {
  return __builtin_bit_cast(float, __builtin_amdgcn_mov_dpp(__builtin_bit_cast(int, x), CTRL, 0xf, 0xf, true));
}
__device__ __forceinline__ float wave_sum(float x) {
  x += dpp<0x128>(x); x += dpp<0x124>(x); x += dpp<0x122>(x); x += dpp<0x121>(x);
  auto s = __builtin_amdgcn_permlane16_swap(__float_as_uint(x), __float_as_uint(x), false, false);
  x = __uint_as_float(s[0]) + __uint_as_float(s[1]);
  auto t = __builtin_amdgcn_permlane32_swap(__float_as_uint(x), __float_as_uint(x), false, false);
  return __uint_as_float(t[0]) + __uint_as_float(t[1]);
}
#define WAVE_FENCE() do { __builtin_amdgcn_fence(__ATOMIC_ACQ_REL, "wavefront"); __builtin_amdgcn_wave_barrier(); } while (0)
#define SB() __builtin_amdgcn_sched_barrier(0)
#define LDS_WAIT() asm volatile("s_waitcnt lgkmcnt(0)" ::: "memory")
#define LDS_BARRIER() asm volatile("s_waitcnt lgkmcnt(0)\n\ts_barrier" ::: "memory")

constexpr int SLOT = 32 * 1024, RING = 3 * SLOT;
constexpr int KZ_OFF = RING, KS_OFF = KZ_OFF + 4096;
constexpr int VS_OFF = KS_OFF + 4096;
constexpr int VZ_OFF = VS_OFF + 2048;
constexpr int Q_OFF = VZ_OFF + 2048;
constexpr int PRIV = Q_OFF + 1024;
constexpr int KT_RS = 144, KT_HALF = 16 * KT_RS, KT_BYTES = 2 * KT_HALF;
constexpr int PT_OFF = KT_BYTES;
constexpr int SC_OFF = PT_OFF + 1024;
constexpr int PRIV_W = SC_OFF + 4096;
constexpr int CTL = PRIV + NCW * PRIV_W;
constexpr int C_FREE = CTL, C_FULL = CTL + 32, C_MX = CTL + 48, C_LC = C_MX + NCW * 16;
constexpr int LDS_BYTES = C_LC + NCW * 16;
static_assert(LDS_BYTES <= 160 * 1024, "LDS");

constexpr int REC = 32 + NSPLIT * DH;
static __device__ __attribute__((aligned(4096))) float part[NBATCH * HQ * REC];

template <int J> __device__ __forceinline__ void drain_rows(unsigned fa) {
  asm volatile("s_waitcnt vmcnt(%0)\n\tds_write_b32 %1, %2" :: "i"(4 * J), "v"(fa), "v"(NLT * 32 - 4 * J) : "memory");
  if constexpr (J > 0) drain_rows<J - 1>(fa);
}

__global__ void __launch_bounds__(NTHR, 2) __attribute__((aligned(8192)))
kv_split(const int* __restrict__ qk, const int* __restrict__ qv,
         const unsigned short* __restrict__ kzr, const unsigned short* __restrict__ ksc,
         const unsigned short* __restrict__ q,
         const unsigned short* __restrict__ vsc, const unsigned short* __restrict__ vzr) {
  __shared__ __attribute__((aligned(16))) unsigned char smem[LDS_BYTES];
  const int tid = threadIdx.x, lane = tid & 63;
  const int wv = __builtin_amdgcn_readfirstlane(tid >> 6);
  const int bid = blockIdx.x;
  int gbase = 0;
#pragma unroll
  for (int k = 0; k < 8; ++k) {
    if (k > 0 && BB_[k] == BB_[k - 1] + 1) continue;
    const int n = (k < 7 && BB_[k + 1] == BB_[k] + 1) ? 3 : 1;
    gbase |= ((bid >> k) & n) << BB_[k];
  }
  const int voff = lane * 16;
  asm volatile("" : "+s"(gbase) : "s"(qk), "s"(qv), "s"(kzr), "s"(ksc), "s"(q), "s"(vsc), "s"(vzr));
  if (wv == 0) {
    __builtin_amdgcn_s_setprio(3);
    const unsigned lb = (unsigned)(size_t)(LAS unsigned char*)smem;
    const unsigned fa = lb + C_FULL, fr = lb + C_FREE;
    const int k7 = voff + ((unsigned)gbase >> 7), k8 = voff + ((unsigned)gbase >> 8);
#define PAR2(M0, VA, P, O0, O1) asm volatile("s_mov_b32 m0, %0\n\ts_nop 0\n\tglobal_load_lds_dwordx4 %1, %2 offset:" #O0 " nt\n\tglobal_load_lds_dwordx4 %1, %2 offset:" #O1 " nt" \
                                          :: "s"(lb + (M0)), "v"(VA), "s"(P) : "memory")
#define PAR1(M0, VA, P, POL) asm volatile("s_mov_b32 m0, %0\n\ts_nop 0\n\tglobal_load_lds_dwordx4 %1, %2" POL :: "s"(lb + (M0)), "v"(VA), "s"(P) : "memory")
    PAR2(KZ_OFF, k7, kzr, 0, 1024); PAR2(KS_OFF, k7, ksc, 0, 1024);
    PAR1(Q_OFF, voff + (gbase >> 22) * 1024, q, "");
    PAR2(KZ_OFF, k7 + 2048, kzr, 2048, 3072); PAR2(KS_OFF, k7 + 2048, ksc, 2048, 3072);
    PAR1(VS_OFF, k8, vsc, " nt"); PAR1(VZ_OFF, k8, vzr, " nt"); PAR1(VS_OFF + 1024, k8 + 2048, vsc, " nt"); PAR1(VZ_OFF + 1024, k8 + 2048, vzr, " nt");
    static_assert(pos2off(127) == 0x3f800 && SB_[2] < SB_[3] && SB_[3] < SB_[4] && SB_[4] < SB_[5] && SB_[5] < SB_[6], "address walk of the loader");
    const int vo1 = voff + pos2off(1) - 1024, vo2 = voff + pos2off(2) - 2048, vo3 = voff + pos2off(3) - 3072;
    unsigned mb = lb;
    int so = gbase, it = 0;
    int cnt = (4 - NFL) & ~3;
    unsigned long long src = (unsigned long long)qk;
    i32x4 f;
    asm volatile("" : "=v"(f));
#define ROW(J, V) "s_waitcnt vmcnt(%9)\n\tbuffer_load_dwordx4 " V ", %6, %7 offen offset:" #J "*1024 nt lds\n\t"
#pragma unroll 1
    do {
      const __amdgpu_buffer_rsrc_t rs = __builtin_amdgcn_make_buffer_rsrc((void*)src, 0, KV_BYTES, 0x00020000);
      asm volatile("s_mov_b32 m0, %1\n\t" ROW(0, "%2") ROW(1, "%3") ROW(2, "%4") ROW(3, "%5") "ds_write_b32 %8, %0\n\tv_add_u32 %0, 4, %0"
                   : "+v"(cnt) : "s"(mb), "v"(voff), "v"(vo1), "v"(vo2), "v"(vo3), "s"(rs), "s"(so), "v"(fa), "i"(NFL - 1) : "memory");
      ++it;
      mb += 4096;
      so = ((so | pos2off(3)) + pos2off(4)) & ~pos2off(3);
      if ((it & 7) == 7) asm volatile("ds_read_b128 %0, %1" : "=v"(f) : "v"(fr) : "memory");
      if ((it & 7) == 0) {
        const int T = it >> 3;
        if (T == 1) LDS_BARRIER();
        if (mb == lb + RING) mb = lb;
        if ((T & 3) == 0) { so = gbase | pos2off((T & 12) << 5); if (T == NT) src = (unsigned long long)qv; }
        asm volatile("s_waitcnt lgkmcnt(0)" : "+v"(f) :: "memory");
        while (T > 2 && T < NLT && __builtin_amdgcn_readfirstlane(min(min(f[0], f[1]), min(f[2], f[3]))) < T - 2) {
          __builtin_amdgcn_s_sleep(1);
          asm volatile("ds_read_b128 %0, %1\n\ts_waitcnt lgkmcnt(0)" : "=v"(f) : "v"(fr) : "memory");
        }
      }
    } while (it < NLT * 8);
    drain_rows<(NFL - 1) / 4>(fa);
    asm volatile("s_waitcnt lgkmcnt(0)\n\ts_endpgm");
  }
  const int w = wv - 1;
  const int lg = lane >> 4, c = lane & 15, r = lane & 31, h = lane >> 5;
  volatile LAS int* ctl = (volatile LAS int*)(smem + CTL);
  if (lane == 0) { ctl[w] = 0; ctl[4 + w] = 0; }
#define POST_STAGE(v) do { LDS_WAIT(); if (lane == 0) ctl[4 + w] = (v); } while (0)
#define WAIT_STAGE(v) do { while ((__builtin_amdgcn_readfirstlane(ctl[4] & ctl[5] & ctl[6] & ctl[7]) & (v)) != (v)) __builtin_amdgcn_s_sleep(1); asm volatile("" ::: "memory"); } while (0)
#define WAIT_ROWS(n) do { while (__builtin_amdgcn_readfirstlane(ctl[8]) < (n)) __builtin_amdgcn_s_sleep(1); asm volatile("" ::: "memory"); } while (0)
  LDS_BARRIER();
  unsigned char* kt = smem + PRIV + w * PRIV_W;
  const unsigned char* ringw = smem + w * 1024 + voff;
  unsigned char* kw = kt + (r >> 4) * KT_HALF + h * KT_RS + (r & 15) * 8;
  const unsigned char* kr = kt + (lg & 1) * KT_HALF + c * KT_RS + (lg >> 1) * 16;
  WAIT_ROWS(4);
  u32x4 Q[4];
#pragma unroll
  for (int s = 0; s < 4; ++s) Q[s] = *(const u32x4*)(smem + Q_OFF + (c >> 2) * (DH * 2) + 128 * (lg & 1) + 16 * (lg >> 1) + 32 * s);
  constexpr float SCALE = 0.088388347648318440f, LOG2E = 1.4426950408889634f;
  float mx = -INFINITY;
  int so = 0;

#pragma unroll 1
  for (int T = 0; T < NT; ++T) {
    const unsigned char* kp = smem + KZ_OFF + pos2g(T << 5) * 256 + r * 8;
    u32x2 kzp[2], ksp[2];
#pragma unroll
    for (int hf = 0; hf < 2; ++hf) { kzp[hf] = *(const u32x2*)(kp + hf * 256); ksp[hf] = *(const u32x2*)(kp + hf * 256 + 4096); }
    WAIT_ROWS(T * 32 + 32);
    u32x4 x[8];
#pragma unroll
    for (int i = 0; i < 8; ++i) x[i] = *(const u32x4*)(ringw + so + i * 4096);
    LDS_WAIT();
    if (lane == 0) ctl[w] = T + 1;
    asm volatile("" : "+v"(x[0]), "+v"(x[1]), "+v"(x[2]), "+v"(x[3]), "+v"(x[4]), "+v"(x[5]), "+v"(x[6]), "+v"(x[7]) :: "memory");
    so = so == 2 * SLOT ? 0 : so + SLOT;
#pragma unroll
    for (int i = 0; i < 8; ++i) {
      const int hf = i >> 2;
      const float kz0 = lo16(kzp[hf][0]), kz1 = hi16(kzp[hf][0]), kz2 = lo16(kzp[hf][1]), kz3 = hi16(kzp[hf][1]);
      const float ks0 = lo16(ksp[hf][0]), ks1 = hi16(ksp[hf][0]), ks2 = lo16(ksp[hf][1]), ks3 = hi16(ksp[hf][1]);
      const unsigned t01 = pk((float)(int)x[i][0] - kz0, (float)(int)x[i][1] - kz1), t23 = pk((float)(int)x[i][2] - kz2, (float)(int)x[i][3] - kz3);
      u32x2 kk;
      kk[0] = pk(lo16(t01) * ks0, hi16(t01) * ks1);
      kk[1] = pk(lo16(t23) * ks2, hi16(t23) * ks3);
      *(u32x2*)(kw + i * 2 * KT_RS) = kk;
    }
    WAVE_FENCE();
    f32x4 d = {0.f, 0.f, 0.f, 0.f};
#pragma unroll
    for (int s = 0; s < 4; ++s) {
      const u32x4 bf = *(const u32x4*)(kr + s * 32);
      d = mfma(Q[s], bf, d);
    }
    WAVE_FENCE();
    const float sc = rbf(rbf(d[0]) * SCALE);
    mx = fmaxf(mx, sc);
    *(float*)(kt + SC_OFF + T * 256 + c * 16 + lg * 4) = sc;
  }
  mx = fmaxf(mx, dpp<0x128>(mx)); mx = fmaxf(mx, dpp<0x124>(mx)); mx = fmaxf(mx, dpp<0x122>(mx)); mx = fmaxf(mx, dpp<0x121>(mx));
  if (c == 0) *(float*)(smem + C_MX + w * 16 + lg * 4) = mx;
  POST_STAGE(1);
  WAIT_STAGE(1);
  f32x4 m = *(const f32x4*)(smem + C_MX);
#pragma unroll
  for (int ww = 1; ww < NCW; ++ww) {
    const f32x4 o = *(const f32x4*)(smem + C_MX + ww * 16);
#pragma unroll
    for (int j = 0; j < 4; ++j) m[j] = fmaxf(m[j], o[j]);
  }
  float lsum[4], cz[4];
  f32x4 acc[4];
#pragma unroll
  for (int j = 0; j < 4; ++j) { lsum[j] = 0.f; cz[j] = 0.f; acc[j] = f32x4{0.f, 0.f, 0.f, 0.f}; }
  const bool live = ((lane >> 3) & 1) == ((lane >> 4) & 1);
  const int lanev = pos2vp((lg << 5) | ((c >> 1) << 2) | w) + (c & 1) * 2;
  unsigned char* pt = kt + PT_OFF;
  static_assert(BB_[3] < 22 && BB_[4] < 22 && BB_[5] < 22, "blockIdx bits 3-5 pick the key split");
  const int split = (bid >> 3) & 7, rec = (bid & 7) | ((bid >> 6) << 3);
  const unsigned fulla = (unsigned)(size_t)(LAS unsigned char*)smem + C_FULL, ringa = (unsigned)(size_t)(LAS unsigned char*)smem + w * 1024 + voff;
  const __amdgpu_buffer_rsrc_t rsV = __builtin_amdgcn_make_buffer_rsrc((void*)qv, 0, KV_BYTES, 0x00020000);
  const int lastb = gbase | pos2off((NT - 1) << 5);
  const int lastv = pos2off(lg) + w * 128 + c * 8;
  u32x2 dr[16];
  auto v_tile = [&](const int T, auto peel_) {
    constexpr bool PEEL = decltype(peel_)::value;
    u32x4 A = *(const u32x4*)(pt + (T & 3) * 256 + h * 128 + (lane & 7) * 16);
    if (!live) A = u32x4{0u, 0u, 0u, 0u};
    u32x4 Bf[4];
#pragma unroll
    for (int mm = 0; mm < 4; ++mm) {
      u32x4 xa, xb;
      while (true) {
        int fv;
        asm volatile("ds_read_b32 %0, %3\n\tds_read_b128 %1, %4 offset:%5\n\tds_read_b128 %2, %4 offset:%6\n\ts_waitcnt lgkmcnt(0)"
                     : "=&v"(fv), "=&v"(xa), "=&v"(xb) : "v"(fulla), "v"(ringa + so), "i"(mm * 8192), "i"(mm * 8192 + 4096) : "memory");
        if (__builtin_amdgcn_readfirstlane(fv) >= (NT + T) * 32 + mm * 8 + 8) break;
        if (!PEEL) __builtin_amdgcn_s_sleep(2);
      }
      if (PEEL && (mm & 1)) {
#pragma unroll
        for (int i = 4 * (mm - 1); i < 4 * (mm + 1); ++i) { dr[i] = __builtin_amdgcn_raw_buffer_load_b64(rsV, lastv + (i & 1) * 512, lastb + pos2off((i >> 1) << 2), 2); SB(); }
      }
      if (mm == 3 && lane == 0) ctl[w] = NT + T + 1;
#pragma unroll
      for (int e = 0; e < 4; ++e) { unsigned t = pk((float)(int)xa[e], (float)(int)xb[e]); asm volatile("" : "+v"(t)); Bf[e][mm] = t; }
    }
    so = so == 2 * SLOT ? 0 : so + SLOT;
#pragma unroll
    for (int e = 0; e < 4; ++e) acc[e] = mfma(A, Bf[e], acc[e]);
    WAVE_FENCE();
  };
#pragma unroll 1
  for (int T = 0; T < NT - 2; ++T) {
    if ((T & 3) == 0) {
      const int tk2 = lanev ^ pos2vp(T << 5);
      const f32x4 S = *(const f32x4*)(kt + SC_OFF + T * 256 + voff);
      const float vs = lo16(*(const unsigned short*)(smem + VS_OFF + tk2));
      const float vz = lo16(*(const unsigned short*)(smem + VZ_OFF + tk2));
      unsigned char* pw = pt + lg * 256 + (c & 1) * 128 + (c >> 1) * 2;
#pragma unroll
      for (int j = 0; j < 4; ++j) {
        const float p = __builtin_amdgcn_exp2f((S[j] - m[j]) * LOG2E);
        const float pv = p * vs;
        const float hi = rbf(pv), lo = rbf(pv - hi);
        lsum[j] += p;
        cz[j] += (hi + lo) * vz;
        const int rt = (j & 1) + 4 * (j >> 1);
        *(unsigned short*)(pw + rt * 16) = (unsigned short)(__builtin_bit_cast(unsigned, hi) >> 16);
        *(unsigned short*)(pw + (rt + 2) * 16) = (unsigned short)(__builtin_bit_cast(unsigned, lo) >> 16);
      }
      if (T == NT - 4) {
        f32x4 lv, cv;
#pragma unroll
        for (int j = 0; j < 4; ++j) { lv[j] = wave_sum(lsum[j]); cv[j] = wave_sum(cz[j]); }
        const float c0 = (lg & 1) ? cv[2] : cv[0], c1 = (lg & 1) ? cv[3] : cv[1];
#pragma unroll
        for (int e = 0; e < 4; ++e) { acc[e][0] -= c0; acc[e][1] -= c1; }
        if (lane == 0) *(f32x4*)(smem + C_LC + w * 16) = lv;
        POST_STAGE(3);
      }
      WAVE_FENCE();
    }
    v_tile(T, BoolC<false>{});
    if (T == NT - 3 && w == 0) {
      WAIT_STAGE(3);
      if (lane < 4) {
        float L = 0.f, M = -INFINITY;
#pragma unroll
        for (int ww = 0; ww < NCW; ++ww) { L += *(const float*)(smem + C_LC + ww * 16 + lane * 4); M = fmaxf(M, *(const float*)(smem + C_MX + ww * 16 + lane * 4)); }
        *(f32x2*)(part + (lane * 32 + rec) * REC + split * 2) = f32x2{M, L};
      }
    }
  }
  v_tile(NT - 2, BoolC<true>{});
  SB();
  {
    f32x4 o0, o1;
#pragma unroll
    for (int e = 0; e < 4; ++e) { o0[e] = acc[e][0] + acc[e][2]; o1[e] = acc[e][1] + acc[e][3]; }
    float* ow = (float*)kt + 2 * (lg & 1) * DH + 64 * (lg >> 1) + 4 * c;
    *(f32x4*)ow = o0;
    *(f32x4*)(ow + DH) = o1;
  }
  LDS_BARRIER();
  f32x2 O = {0.f, 0.f};
#pragma unroll
  for (int ww = 0; ww < NCW; ++ww) O += *(const f32x2*)(smem + PRIV + ww * PRIV_W + (lg * DH + 32 * w + 2 * c) * 4);
  const unsigned char* pa = smem + PRIV + lg * PRIV_W + PT_OFF + 3 * 256 + (((c >> 2) & 1) + 2 * (c & 1) + 4 * (c >> 3)) * 16;
  const u32x4 A0 = *(const u32x4*)pa, A1 = *(const u32x4*)(pa + 128);
  f32x4 d0 = {O[0], 0.f, 0.f, 0.f}, d1 = {O[1], 0.f, 0.f, 0.f};
  unsigned oo = ((lg * 32 + rec) * REC + 32 + split * DH + 32 * w + 2 * c) * 4;
  asm volatile("" : "+v"(d0), "+v"(d1), "+v"(oo));
  SB();
  u32x4 B[2][2];
#pragma unroll
  for (int mm = 0; mm < 4; ++mm) {
#pragma unroll
    for (int ks = 0; ks < 2; ++ks)
#pragma unroll
      for (int e = 0; e < 2; ++e) B[ks][e][mm] = pk((float)(int)dr[4 * mm + ks][e], (float)(int)dr[4 * mm + 2 + ks][e]);
    SB();
  }
  d0 = mfma(A0, B[0][0], d0); d1 = mfma(A0, B[0][1], d1);
  d0 = mfma(A1, B[1][0], d0); d1 = mfma(A1, B[1][1], d1);
  *(f32x2*)((char*)part + oo) = f32x2{d0[0] + d0[1], d1[0] + d1[1]};
}

__global__ void __launch_bounds__(64)
kv_combine(unsigned* __restrict__ out) {
  const char* rec = (const char*)(part + blockIdx.x * REC);
  const f32x2 ml = *(const f32x2*)(rec + (threadIdx.x & 7) * 8);
  f32x2 ov[NSPLIT];
#pragma unroll
  for (int s = 0; s < NSPLIT; ++s) ov[s] = *(const f32x2*)(rec + (threadIdx.x * 8 + 128 + s * 512));
  SB();
  int bh = 0;
#pragma unroll
  for (int k = 0; k < 5; ++k) bh |= ((blockIdx.x >> k) & 1) << (BB_[k < 3 ? k : k + 3] - 22);
  out += (bh * 4 + (blockIdx.x >> 5)) * (DH / 2);
  float M = fmaxf(ml[0], dpp<0xB1>(ml[0]));
  M = fmaxf(M, dpp<0x4E>(M)); M = fmaxf(M, dpp<0x141>(M));
  float wt = __builtin_amdgcn_exp2f((ml[0] - M) * 1.4426950408889634f);
  float L = wt * ml[1];
  L += dpp<0xB1>(L); L += dpp<0x4E>(L); L += dpp<0x141>(L);
  wt *= __builtin_amdgcn_rcpf(L);
  f32x2 O = {0.f, 0.f};
#pragma unroll
  for (int s = 0; s < NSPLIT; ++s) O += ov[s] * __builtin_bit_cast(float, __builtin_amdgcn_readlane(__builtin_bit_cast(int, wt), s));
  out[threadIdx.x] = pk(O[0], O[1]);
}
}

extern "C" void kernel_launch(void* const* d_in, const int* in_sizes, int n_in, void* d_out, int out_size,
                              void* d_ws, size_t ws_size, hipStream_t stream) {
  const unsigned short* q = (const unsigned short*)d_in[0];
  const int* qk = (const int*)d_in[1];
  const int* qv = (const int*)d_in[2];
  const unsigned short* ks = (const unsigned short*)d_in[3];
  const unsigned short* kz = (const unsigned short*)d_in[4];
  const unsigned short* vs = (const unsigned short*)d_in[5];
  const unsigned short* vz = (const unsigned short*)d_in[6];
  kv_split<<<NBH * NSPLIT, NTHR, 0, stream>>>(qk, qv, kz, ks, q, vs, vz);
  kv_combine<<<NBATCH * HQ, 64, 0, stream>>>((unsigned*)d_out);
}
```

```cpp
#include <hip/hip_runtime.h>
#include <hip/hip_bf16.h>
#include <stdint.h>

namespace {
constexpr int NBATCH = 4, HQ = 32, HKV = 8, SKV = 8192, DH = 128;
constexpr int NBH = NBATCH * HKV;
constexpr int NSPLIT = 8;
constexpr int NCW = 4;
constexpr int NT = 16;
constexpr int NLT = 2 * NT - 1;
constexpr int NTHR = (NCW + 1) * 64;
constexpr int KV_BYTES = NBH * SKV * DH * 4;
constexpr int NFL = 22;

constexpr int SB_[9] = {13, 14, 11, 12, 15, 16, 17, 10, 19};
constexpr int BB_[8] = {23, 22, 25, 18, 20, 21, 24, 26};
__host__ __device__ constexpr int pos2off(int x) { int o = 0; for (int k = 0; k < 9; ++k) o |= ((x >> k) & 1) << SB_[k]; return o; }
__host__ __device__ constexpr int pos2g(int x) { return ((pos2off(x) >> 15) & 7) | ((pos2off(x) >> 16) & 8); }
__host__ __device__ constexpr int pos2vp(int x) { return pos2g(x) * 128 + ((pos2off(x) >> 8) & 0x7c); }
static_assert(pos2off(511) == 0xbfc00, "a workgroup covers 16 whole groups");
static_assert(pos2g(15) == 0 && pos2g(16) == 1, "rows 0-3 and 4-7 of a tile lie in two neighbouring groups");

typedef unsigned u32x4 __attribute__((ext_vector_type(4)));
typedef unsigned u32x2 __attribute__((ext_vector_type(2)));
typedef int i32x4 __attribute__((ext_vector_type(4)));
typedef float f32x4 __attribute__((ext_vector_type(4)));
typedef float f32x2 __attribute__((ext_vector_type(2)));
typedef __bf16 bf16x2 __attribute__((ext_vector_type(2)));
typedef __bf16 bf16x8 __attribute__((ext_vector_type(8)));
template <bool B> struct BoolC { static constexpr bool value = B; };
#define LAS __attribute__((address_space(3)))

__device__ __forceinline__ unsigned pk(float lo, float hi) {
  bf16x2 v = {(__bf16)lo, (__bf16)hi};
  return __builtin_bit_cast(unsigned, v);
}
__device__ __forceinline__ float lo16(unsigned u) { return __builtin_bit_cast(float, u << 16); }
__device__ __forceinline__ float hi16(unsigned u) { return __builtin_bit_cast(float, u & 0xffff0000u); }
__device__ __forceinline__ float rbf(float x) { return (float)(__bf16)x; }
__device__ __forceinline__ f32x4 mfma(u32x4 a, u32x4 b, f32x4 c) {
  return __builtin_amdgcn_mfma_f32_16x16x32_bf16(__builtin_bit_cast(bf16x8, a), __builtin_bit_cast(bf16x8, b), c, 0, 0, 0);
}
template <int CTRL> __device__ __forceinline__ float dpp(float x) {
  return __builtin_bit_cast(float, __builtin_amdgcn_mov_dpp(__builtin_bit_cast(int, x), CTRL, 0xf, 0xf, true));
}
__device__ __forceinline__ float wave_sum(float x) {
  x += dpp<0x128>(x); x += dpp<0x124>(x); x += dpp<0x122>(x); x += dpp<0x121>(x);
  auto s = __builtin_amdgcn_permlane16_swap(__float_as_uint(x), __float_as_uint(x), false, false);
  x = __uint_as_float(s[0]) + __uint_as_float(s[1]);
  auto t = __builtin_amdgcn_permlane32_swap(__float_as_uint(x), __float_as_uint(x), false, false);
  return __uint_as_float(t[0]) + __uint_as_float(t[1]);
}
#define WAVE_FENCE() do { __builtin_amdgcn_fence(__ATOMIC_ACQ_REL, "wavefront"); __builtin_amdgcn_wave_barrier(); } while (0)
#define SB() __builtin_amdgcn_sched_barrier(0)
#define LDS_WAIT() asm volatile("s_waitcnt lgkmcnt(0)" ::: "memory")
#define LDS_BARRIER() asm volatile("s_waitcnt lgkmcnt(0)\n\ts_barrier" ::: "memory")

constexpr int SLOT = 32 * 1024, RING = 3 * SLOT;
constexpr int KZ_OFF = RING, KS_OFF = KZ_OFF + 4096;
constexpr int VS_OFF = KS_OFF + 4096;
constexpr int VZ_OFF = VS_OFF + 2048;
constexpr int Q_OFF = VZ_OFF + 2048;
constexpr int PRIV = Q_OFF + 1024;
constexpr int KT_RS = 144, KT_HALF = 16 * KT_RS, KT_BYTES = 2 * KT_HALF;
constexpr int PT_OFF = KT_BYTES;
constexpr int SC_OFF = PT_OFF + 1024;
constexpr int PRIV_W = SC_OFF + 4096;
constexpr int CTL = PRIV + NCW * PRIV_W;
constexpr int C_FREE = CTL, C_FULL = CTL + 32, C_MX = CTL + 48, C_LC = C_MX + NCW * 16;
constexpr int LDS_BYTES = C_LC + NCW * 16;
static_assert(LDS_BYTES <= 160 * 1024, "LDS");

constexpr int REC = 32 + NSPLIT * DH;
static __device__ __attribute__((aligned(4096))) float part[NBATCH * HQ * REC];

template <int J> __device__ __forceinline__ void drain_rows(unsigned fa) {
  asm volatile("s_waitcnt vmcnt(%0)\n\tds_write_b32 %1, %2" :: "i"(4 * J), "v"(fa), "v"(NLT * 32 - 4 * J) : "memory");
  if constexpr (J > 0) drain_rows<J - 1>(fa);
}

__global__ void __launch_bounds__(NTHR, 2) __attribute__((aligned(8192)))
kv_split(const int* __restrict__ qk, const int* __restrict__ qv,
         const unsigned short* __restrict__ kzr, const unsigned short* __restrict__ ksc,
         const unsigned short* __restrict__ q,
         const unsigned short* __restrict__ vsc, const unsigned short* __restrict__ vzr) {
  __shared__ __attribute__((aligned(16))) unsigned char smem[LDS_BYTES];
  const int tid = threadIdx.x, lane = tid & 63;
  const int wv = __builtin_amdgcn_readfirstlane(tid >> 6);
  const int bid = blockIdx.x;
  int gbase = 0;
#pragma unroll
  for (int k = 0; k < 8; ++k) {
    if (k > 0 && BB_[k] == BB_[k - 1] + 1) continue;
    const int n = (k < 7 && BB_[k + 1] == BB_[k] + 1) ? 3 : 1;
    gbase |= ((bid >> k) & n) << BB_[k];
  }
  const int voff = lane * 16;
  asm volatile("" : "+s"(gbase) : "s"(qk), "s"(qv), "s"(kzr), "s"(ksc), "s"(q), "s"(vsc), "s"(vzr));
  if (wv == 0) {
    __builtin_amdgcn_s_setprio(3);
    const unsigned lb = (unsigned)(size_t)(LAS unsigned char*)smem;
    const unsigned fa = lb + C_FULL, fr = lb + C_FREE;
    const int k7 = voff + ((unsigned)gbase >> 7), k8 = voff + ((unsigned)gbase >> 8);
#define PAR2(M0, VA, P, O0, O1) asm volatile("s_mov_b32 m0, %0\n\ts_nop 0\n\tglobal_load_lds_dwordx4 %1, %2 offset:" #O0 " nt\n\tglobal_load_lds_dwordx4 %1, %2 offset:" #O1 " nt" \
                                          :: "s"(lb + (M0)), "v"(VA), "s"(P) : "memory")
#define PAR1(M0, VA, P, POL) asm volatile("s_mov_b32 m0, %0\n\ts_nop 0\n\tglobal_load_lds_dwordx4 %1, %2" POL :: "s"(lb + (M0)), "v"(VA), "s"(P) : "memory")
    PAR2(KZ_OFF, k7, kzr, 0, 1024); PAR2(KS_OFF, k7, ksc, 0, 1024);
    PAR1(Q_OFF, voff + (gbase >> 22) * 1024, q, "");
    PAR2(KZ_OFF, k7 + 2048, kzr, 2048, 3072); PAR2(KS_OFF, k7 + 2048, ksc, 2048, 3072);
    PAR1(VS_OFF, k8, vsc, " nt"); PAR1(VZ_OFF, k8, vzr, " nt"); PAR1(VS_OFF + 1024, k8 + 2048, vsc, " nt"); PAR1(VZ_OFF + 1024, k8 + 2048, vzr, " nt");
    static_assert(pos2off(127) == 0x3f800 && SB_[2] < SB_[3] && SB_[3] < SB_[4] && SB_[4] < SB_[5] && SB_[5] < SB_[6], "address walk of the loader");
    const int vo1 = voff + pos2off(1) - 1024, vo2 = voff + pos2off(2) - 2048, vo3 = voff + pos2off(3) - 3072;
    unsigned mb = lb;
    int so = gbase, it = 0;
    int cnt = (4 - NFL) & ~3;
    unsigned long long src = (unsigned long long)qk;
    i32x4 f;
    asm volatile("" : "=v"(f));
#define ROW(J, V) "s_waitcnt vmcnt(%9)\n\tbuffer_load_dwordx4 " V ", %6, %7 offen offset:" #J "*1024 nt lds\n\t"
#pragma unroll 1
    do {
      const __amdgpu_buffer_rsrc_t rs = __builtin_amdgcn_make_buffer_rsrc((void*)src, 0, KV_BYTES, 0x00020000);
      asm volatile("s_mov_b32 m0, %1\n\t" ROW(0, "%2") ROW(1, "%3") ROW(2, "%4") ROW(3, "%5") "ds_write_b32 %8, %0\n\tv_add_u32 %0, 4, %0\n\ts_nop 4"
                   : "+v"(cnt) : "s"(mb), "v"(voff), "v"(vo1), "v"(vo2), "v"(vo3), "s"(rs), "s"(so), "v"(fa), "i"(NFL - 1) : "memory");
      ++it;
      mb += 4096;
      so = ((so | pos2off(3)) + pos2off(4)) & ~pos2off(3);
      if ((it & 7) == 7) asm volatile("ds_read_b128 %0, %1" : "=v"(f) : "v"(fr) : "memory");
      if ((it & 7) == 0) {
        const int T = it >> 3;
        if (T == 1) LDS_BARRIER();
        if (mb == lb + RING) mb = lb;
        if ((T & 3) == 0) { so = gbase | pos2off((T & 12) << 5); if (T == NT) src = (unsigned long long)qv; }
        asm volatile("s_waitcnt lgkmcnt(1)" : "+v"(f) :: "memory");
        while (T > 2 && T < NLT && __builtin_amdgcn_readfirstlane(min(min(f[0], f[1]), min(f[2], f[3]))) < T - 2) {
          __builtin_amdgcn_s_sleep(1);
          asm volatile("ds_read_b128 %0, %1\n\ts_waitcnt lgkmcnt(0)" : "=v"(f) : "v"(fr) : "memory");
        }
      }
    } while (it < NLT * 8);
    drain_rows<(NFL - 1) / 4>(fa);
    asm volatile("s_waitcnt lgkmcnt(0)\n\ts_endpgm");
  }
  const int w = wv - 1;
  const int lg = lane >> 4, c = lane & 15, r = lane & 31, h = lane >> 5;
  volatile LAS int* ctl = (volatile LAS int*)(smem + CTL);
  if (lane == 0) { ctl[w] = 0; ctl[4 + w] = 0; }
#define POST_STAGE(v) do { LDS_WAIT(); if (lane == 0) ctl[4 + w] = (v); } while (0)
#define WAIT_STAGE(v) do { while ((__builtin_amdgcn_readfirstlane(ctl[4] & ctl[5] & ctl[6] & ctl[7]) & (v)) != (v)) __builtin_amdgcn_s_sleep(1); asm volatile("" ::: "memory"); } while (0)
#define WAIT_ROWS(n) do { while (__builtin_amdgcn_readfirstlane(ctl[8]) < (n)) __builtin_amdgcn_s_sleep(1); asm volatile("" ::: "memory"); } while (0)
  LDS_BARRIER();
  unsigned char* kt = smem + PRIV + w * PRIV_W;
  const unsigned char* ringw = smem + w * 1024 + voff;
  unsigned char* kw = kt + (r >> 4) * KT_HALF + h * KT_RS + (r & 15) * 8;
  const unsigned char* kr = kt + (lg & 1) * KT_HALF + c * KT_RS + (lg >> 1) * 16;
  WAIT_ROWS(4);
  u32x4 Q[4];
#pragma unroll
  for (int s = 0; s < 4; ++s) Q[s] = *(const u32x4*)(smem + Q_OFF + (c >> 2) * (DH * 2) + 128 * (lg & 1) + 16 * (lg >> 1) + 32 * s);
  constexpr float SCALE = 0.088388347648318440f, LOG2E = 1.4426950408889634f;
  float mx = -INFINITY;
  int so = 0;

#pragma unroll 1
  for (int T = 0; T < NT; ++T) {
    const unsigned char* kp = smem + KZ_OFF + pos2g(T << 5) * 256 + r * 8;
    u32x2 kzp[2], ksp[2];
#pragma unroll
    for (int hf = 0; hf < 2; ++hf) { kzp[hf] = *(const u32x2*)(kp + hf * 256); ksp[hf] = *(const u32x2*)(kp + hf * 256 + 4096); }
    WAIT_ROWS(T * 32 + 32);
    u32x4 x[8];
#pragma unroll
    for (int i = 0; i < 8; ++i) x[i] = *(const u32x4*)(ringw + so + i * 4096);
    LDS_WAIT();
    if (lane == 0) ctl[w] = T + 1;
    asm volatile("" : "+v"(x[0]), "+v"(x[1]), "+v"(x[2]), "+v"(x[3]), "+v"(x[4]), "+v"(x[5]), "+v"(x[6]), "+v"(x[7]) :: "memory");
    so = so == 2 * SLOT ? 0 : so + SLOT;
#pragma unroll
    for (int i = 0; i < 8; ++i) {
      const int hf = i >> 2;
      const float kz0 = lo16(kzp[hf][0]), kz1 = hi16(kzp[hf][0]), kz2 = lo16(kzp[hf][1]), kz3 = hi16(kzp[hf][1]);
      const float ks0 = lo16(ksp[hf][0]), ks1 = hi16(ksp[hf][0]), ks2 = lo16(ksp[hf][1]), ks3 = hi16(ksp[hf][1]);
      const unsigned t01 = pk((float)(int)x[i][0] - kz0, (float)(int)x[i][1] - kz1), t23 = pk((float)(int)x[i][2] - kz2, (float)(int)x[i][3] - kz3);
      u32x2 kk;
      kk[0] = pk(lo16(t01) * ks0, hi16(t01) * ks1);
      kk[1] = pk(lo16(t23) * ks2, hi16(t23) * ks3);
      *(u32x2*)(kw + i * 2 * KT_RS) = kk;
    }
    WAVE_FENCE();
    f32x4 d = {0.f, 0.f, 0.f, 0.f};
#pragma unroll
    for (int s = 0; s < 4; ++s) {
      const u32x4 bf = *(const u32x4*)(kr + s * 32);
      d = mfma(Q[s], bf, d);
    }
    WAVE_FENCE();
    const float sc = rbf(rbf(d[0]) * SCALE);
    mx = fmaxf(mx, sc);
    *(float*)(kt + SC_OFF + T * 256 + c * 16 + lg * 4) = sc;
  }
  mx = fmaxf(mx, dpp<0x128>(mx)); mx = fmaxf(mx, dpp<0x124>(mx)); mx = fmaxf(mx, dpp<0x122>(mx)); mx = fmaxf(mx, dpp<0x121>(mx));
  if (c == 0) *(float*)(smem + C_MX + w * 16 + lg * 4) = mx;
  POST_STAGE(1);
  WAIT_STAGE(1);
  f32x4 m = *(const f32x4*)(smem + C_MX);
#pragma unroll
  for (int ww = 1; ww < NCW; ++ww) {
    const f32x4 o = *(const f32x4*)(smem + C_MX + ww * 16);
#pragma unroll
    for (int j = 0; j < 4; ++j) m[j] = fmaxf(m[j], o[j]);
  }
  float lsum[4], cz[4];
  f32x4 acc[4];
#pragma unroll
  for (int j = 0; j < 4; ++j) { lsum[j] = 0.f; cz[j] = 0.f; acc[j] = f32x4{0.f, 0.f, 0.f, 0.f}; }
  const bool live = ((lane >> 3) & 1) == ((lane >> 4) & 1);
  const int lanev = pos2vp((lg << 5) | ((c >> 1) << 2) | w) + (c & 1) * 2;
  unsigned char* pt = kt + PT_OFF;
  static_assert(BB_[3] < 22 && BB_[4] < 22 && BB_[5] < 22, "blockIdx bits 3-5 pick the key split");
  const int split = (bid >> 3) & 7, rec = (bid & 7) | ((bid >> 6) << 3);
  const unsigned fulla = (unsigned)(size_t)(LAS unsigned char*)smem + C_FULL, ringa = (unsigned)(size_t)(LAS unsigned char*)smem + w * 1024 + voff;
  const __amdgpu_buffer_rsrc_t rsV = __builtin_amdgcn_make_buffer_rsrc((void*)qv, 0, KV_BYTES, 0x00020000);
  const int lastb = gbase | pos2off((NT - 1) << 5);
  const int lastv = pos2off(lg) + w * 128 + c * 8;
  u32x2 dr[16];
  auto v_tile = [&](const int T, auto peel_) {
    constexpr bool PEEL = decltype(peel_)::value;
    u32x4 A = *(const u32x4*)(pt + (T & 3) * 256 + h * 128 + (lane & 7) * 16);
    if (!live) A = u32x4{0u, 0u, 0u, 0u};
    u32x4 Bf[4];
#pragma unroll
    for (int mm = 0; mm < 4; ++mm) {
      u32x4 xa, xb;
      while (true) {
        int fv;
        asm volatile("ds_read_b32 %0, %3\n\tds_read_b128 %1, %4 offset:%5\n\tds_read_b128 %2, %4 offset:%6\n\ts_waitcnt lgkmcnt(0)"
                     : "=&v"(fv), "=&v"(xa), "=&v"(xb) : "v"(fulla), "v"(ringa + so), "i"(mm * 8192), "i"(mm * 8192 + 4096) : "memory");
        if (__builtin_amdgcn_readfirstlane(fv) >= (NT + T) * 32 + mm * 8 + 8) break;
        if (!PEEL) __builtin_amdgcn_s_sleep(2);
      }
      if (PEEL && (mm & 1) == 0) {
#pragma unroll
        for (int i = 4 * mm; i < 4 * mm + 8; ++i) { dr[i] = __builtin_amdgcn_raw_buffer_load_b64(rsV, lastv + (i & 1) * 512, lastb + pos2off((i >> 1) << 2), 2); SB(); }
      }
      if (mm == 3 && lane == 0) ctl[w] = NT + T + 1;
#pragma unroll
      for (int e = 0; e < 4; ++e) { unsigned t = pk((float)(int)xa[e], (float)(int)xb[e]); asm volatile("" : "+v"(t)); Bf[e][mm] = t; }
    }
    so = so == 2 * SLOT ? 0 : so + SLOT;
#pragma unroll
    for (int e = 0; e < 4; ++e) acc[e] = mfma(A, Bf[e], acc[e]);
    WAVE_FENCE();
  };
#pragma unroll 1
  for (int T = 0; T < NT - 2; ++T) {
    if ((T & 3) == 0) {
      const int tk2 = lanev ^ pos2vp(T << 5);
      const f32x4 S = *(const f32x4*)(kt + SC_OFF + T * 256 + voff);
      const float vs = lo16(*(const unsigned short*)(smem + VS_OFF + tk2));
      const float vz = lo16(*(const unsigned short*)(smem + VZ_OFF + tk2));
      unsigned char* pw = pt + lg * 256 + (c & 1) * 128 + (c >> 1) * 2;
#pragma unroll
      for (int j = 0; j < 4; ++j) {
        const float p = __builtin_amdgcn_exp2f((S[j] - m[j]) * LOG2E);
        const float pv = p * vs;
        const float hi = rbf(pv), lo = rbf(pv - hi);
        lsum[j] += p;
        cz[j] += (hi + lo) * vz;
        const int rt = (j & 1) + 4 * (j >> 1);
        *(unsigned short*)(pw + rt * 16) = (unsigned short)(__builtin_bit_cast(unsigned, hi) >> 16);
        *(unsigned short*)(pw + (rt + 2) * 16) = (unsigned short)(__builtin_bit_cast(unsigned, lo) >> 16);
      }
      if (T == NT - 4) {
        f32x4 lv, cv;
#pragma unroll
        for (int j = 0; j < 4; ++j) { lv[j] = wave_sum(lsum[j]); cv[j] = wave_sum(cz[j]); }
        const float c0 = (lg & 1) ? cv[2] : cv[0], c1 = (lg & 1) ? cv[3] : cv[1];
#pragma unroll
        for (int e = 0; e < 4; ++e) { acc[e][0] -= c0; acc[e][1] -= c1; }
        if (lane == 0) *(f32x4*)(smem + C_LC + w * 16) = lv;
        POST_STAGE(3);
      }
      WAVE_FENCE();
    }
    v_tile(T, BoolC<false>{});
    if (T == NT - 3 && w == 0) {
      WAIT_STAGE(3);
      if (lane < 4) {
        float L = 0.f, M = -INFINITY;
#pragma unroll
        for (int ww = 0; ww < NCW; ++ww) { L += *(const float*)(smem + C_LC + ww * 16 + lane * 4); M = fmaxf(M, *(const float*)(smem + C_MX + ww * 16 + lane * 4)); }
        *(f32x2*)(part + (lane * 32 + rec) * REC + split * 2) = f32x2{M, L};
      }
    }
  }
  v_tile(NT - 2, BoolC<true>{});
  SB();
  {
    f32x4 o0, o1;
#pragma unroll
    for (int e = 0; e < 4; ++e) { o0[e] = acc[e][0] + acc[e][2]; o1[e] = acc[e][1] + acc[e][3]; }
    float* ow = (float*)kt + 2 * (lg & 1) * DH + 64 * (lg >> 1) + 4 * c;
    *(f32x4*)ow = o0;
    *(f32x4*)(ow + DH) = o1;
  }
  LDS_BARRIER();
  f32x2 O = {0.f, 0.f};
#pragma unroll
  for (int ww = 0; ww < NCW; ++ww) O += *(const f32x2*)(smem + PRIV + ww * PRIV_W + (lg * DH + 32 * w + 2 * c) * 4);
  const unsigned char* pa = smem + PRIV + lg * PRIV_W + PT_OFF + 3 * 256 + (((c >> 2) & 1) + 2 * (c & 1) + 4 * (c >> 3)) * 16;
  const u32x4 A0 = *(const u32x4*)pa, A1 = *(const u32x4*)(pa + 128);
  f32x4 d0 = {O[0], 0.f, 0.f, 0.f}, d1 = {O[1], 0.f, 0.f, 0.f};
  unsigned oo = ((lg * 32 + rec) * REC + 32 + split * DH + 32 * w + 2 * c) * 4;
  asm volatile("" : "+v"(d0), "+v"(d1), "+v"(oo));
  SB();
  u32x4 B[2][2];
#pragma unroll
  for (int mm = 0; mm < 4; ++mm) {
#pragma unroll
    for (int ks = 0; ks < 2; ++ks)
#pragma unroll
      for (int e = 0; e < 2; ++e) B[ks][e][mm] = pk((float)(int)dr[4 * mm + ks][e], (float)(int)dr[4 * mm + 2 + ks][e]);
    SB();
  }
  d0 = mfma(A0, B[0][0], d0); d1 = mfma(A0, B[0][1], d1);
  d0 = mfma(A1, B[1][0], d0); d1 = mfma(A1, B[1][1], d1);
  *(f32x2*)((char*)part + oo) = f32x2{d0[0] + d0[1], d1[0] + d1[1]};
}

__global__ void __launch_bounds__(64)
kv_combine(unsigned* __restrict__ out) {
  const char* rec = (const char*)(part + blockIdx.x * REC);
  const f32x2 ml = *(const f32x2*)(rec + (threadIdx.x & 7) * 8);
  f32x2 ov[NSPLIT];
#pragma unroll
  for (int s = 0; s < NSPLIT; ++s) ov[s] = *(const f32x2*)(rec + (threadIdx.x * 8 + 128 + s * 512));
  SB();
  int bh = 0;
#pragma unroll
  for (int k = 0; k < 5; ++k) bh |= ((blockIdx.x >> k) & 1) << (BB_[k < 3 ? k : k + 3] - 22);
  out += (bh * 4 + (blockIdx.x >> 5)) * (DH / 2);
  float M = fmaxf(ml[0], dpp<0xB1>(ml[0]));
  M = fmaxf(M, dpp<0x4E>(M)); M = fmaxf(M, dpp<0x141>(M));
  float wt = __builtin_amdgcn_exp2f((ml[0] - M) * 1.4426950408889634f);
  float L = wt * ml[1];
  L += dpp<0xB1>(L); L += dpp<0x4E>(L); L += dpp<0x141>(L);
  wt *= __builtin_amdgcn_rcpf(L);
  f32x2 O = {0.f, 0.f};
#pragma unroll
  for (int s = 0; s < NSPLIT; ++s) O += ov[s] * __builtin_bit_cast(float, __builtin_amdgcn_readlane(__builtin_bit_cast(int, wt), s));
  out[threadIdx.x] = pk(O[0], O[1]);
}
}

extern "C" void kernel_launch(void* const* d_in, const int* in_sizes, int n_in, void* d_out, int out_size,
                              void* d_ws, size_t ws_size, hipStream_t stream) {
  const unsigned short* q = (const unsigned short*)d_in[0];
  const int* qk = (const int*)d_in[1];
  const int* qv = (const int*)d_in[2];
  const unsigned short* ks = (const unsigned short*)d_in[3];
  const unsigned short* kz = (const unsigned short*)d_in[4];
  const unsigned short* vs = (const unsigned short*)d_in[5];
  const unsigned short* vz = (const unsigned short*)d_in[6];
  kv_split<<<NBH * NSPLIT, NTHR, 0, stream>>>(qk, qv, kz, ks, q, vs, vz);
  kv_combine<<<NBATCH * HQ, 64, 0, stream>>>((unsigned*)d_out);
}
```

```cpp
#include <hip/hip_runtime.h>
#include <hip/hip_bf16.h>
#include <stdint.h>

namespace {
constexpr int NBATCH = 4, HQ = 32, HKV = 8, SKV = 8192, DH = 128;
constexpr int NBH = NBATCH * HKV;
constexpr int NSPLIT = 8;
constexpr int NCW = 4;
constexpr int NT = 16;
constexpr int NLT = 2 * NT - 1;
constexpr int NTHR = (NCW + 1) * 64;
constexpr int KV_BYTES = NBH * SKV * DH * 4;
constexpr int NFL = 22;

constexpr int SB_[9] = {13, 14, 11, 12, 15, 16, 17, 10, 19};
constexpr int BB_[8] = {23, 22, 25, 18, 20, 21, 24, 26};
__host__ __device__ constexpr int pos2off(int x) { int o = 0; for (int k = 0; k < 9; ++k) o |= ((x >> k) & 1) << SB_[k]; return o; }
__host__ __device__ constexpr int pos2g(int x) { return ((pos2off(x) >> 15) & 7) | ((pos2off(x) >> 16) & 8); }
__host__ __device__ constexpr int pos2vp(int x) { return pos2g(x) * 128 + ((pos2off(x) >> 8) & 0x7c); }
static_assert(pos2off(511) == 0xbfc00, "a workgroup covers 16 whole groups");
static_assert(pos2g(15) == 0 && pos2g(16) == 1, "rows 0-3 and 4-7 of a tile lie in two neighbouring groups");

typedef unsigned u32x4 __attribute__((ext_vector_type(4)));
typedef unsigned u32x2 __attribute__((ext_vector_type(2)));
typedef int i32x4 __attribute__((ext_vector_type(4)));
typedef float f32x4 __attribute__((ext_vector_type(4)));
typedef float f32x2 __attribute__((ext_vector_type(2)));
typedef __bf16 bf16x2 __attribute__((ext_vector_type(2)));
typedef __bf16 bf16x8 __attribute__((ext_vector_type(8)));
template <bool B> struct BoolC { static constexpr bool value = B; };
#define LAS __attribute__((address_space(3)))

__device__ __forceinline__ unsigned pk(float lo, float hi) {
  bf16x2 v = {(__bf16)lo, (__bf16)hi};
  return __builtin_bit_cast(unsigned, v);
}
__device__ __forceinline__ float lo16(unsigned u) { return __builtin_bit_cast(float, u << 16); }
__device__ __forceinline__ float hi16(unsigned u) { return __builtin_bit_cast(float, u & 0xffff0000u); }
__device__ __forceinline__ float rbf(float x) { return (float)(__bf16)x; }
__device__ __forceinline__ f32x4 mfma(u32x4 a, u32x4 b, f32x4 c) {
  return __builtin_amdgcn_mfma_f32_16x16x32_bf16(__builtin_bit_cast(bf16x8, a), __builtin_bit_cast(bf16x8, b), c, 0, 0, 0);
}
template <int CTRL> __device__ __forceinline__ float dpp(float x) {
  return __builtin_bit_cast(float, __builtin_amdgcn_mov_dpp(__builtin_bit_cast(int, x), CTRL, 0xf, 0xf, true));
}
__device__ __forceinline__ float wave_sum4(const float (&x)[4]) {
  auto s = __builtin_amdgcn_permlane32_swap(__float_as_uint(x[0]), __float_as_uint(x[2]), false, false);
  auto t = __builtin_amdgcn_permlane32_swap(__float_as_uint(x[1]), __float_as_uint(x[3]), false, false);
  auto u = __builtin_amdgcn_permlane16_swap(__float_as_uint(__uint_as_float(s[0]) + __uint_as_float(s[1])), __float_as_uint(__uint_as_float(t[0]) + __uint_as_float(t[1])), false, false);
  float y = __uint_as_float(u[0]) + __uint_as_float(u[1]);
  y += dpp<0x128>(y); y += dpp<0x124>(y); y += dpp<0x122>(y); y += dpp<0x121>(y);
  return y;
}
#define WAVE_FENCE() do { __builtin_amdgcn_fence(__ATOMIC_ACQ_REL, "wavefront"); __builtin_amdgcn_wave_barrier(); } while (0)
#define SB() __builtin_amdgcn_sched_barrier(0)
#define LDS_WAIT() asm volatile("s_waitcnt lgkmcnt(0)" ::: "memory")
#define LDS_BARRIER() asm volatile("s_waitcnt lgkmcnt(0)\n\ts_barrier" ::: "memory")

constexpr int SLOT = 32 * 1024, RING = 3 * SLOT;
constexpr int KZ_OFF = RING, KS_OFF = KZ_OFF + 4096;
constexpr int VS_OFF = KS_OFF + 4096;
constexpr int VZ_OFF = VS_OFF + 2048;
constexpr int Q_OFF = VZ_OFF + 2048;
constexpr int OG_OFF = Q_OFF + 1024;
constexpr int PRIV = OG_OFF + 2048;
constexpr int KT_RS = 144, KT_HALF = 16 * KT_RS, KT_BYTES = 2 * KT_HALF;
constexpr int PT_OFF = KT_BYTES;
constexpr int SC_OFF = PT_OFF + 1024;
constexpr int PRIV_W = SC_OFF + 4096;
constexpr int CTL = PRIV + NCW * PRIV_W;
constexpr int C_FREE = CTL, C_FULL = CTL + 32, C_MX = CTL + 48, C_LC = C_MX + NCW * 16;
constexpr int C_CZ = C_LC + NCW * 16;
constexpr int LDS_BYTES = C_CZ + NCW * 16;
static_assert(LDS_BYTES <= 160 * 1024, "LDS");

constexpr int X_CNT = 0, X_ECHO = 128, X_TAG = 256, X_ML = 384, X_SLOT = 1024, XREC = X_SLOT + NSPLIT * 2048;
static __device__ __attribute__((aligned(4096))) unsigned char xch[NBH * XREC];
constexpr int OOB = 0x40000000, SC1 = 16;
constexpr unsigned SPIN_CAP = 1u << 18;
#define TOUT 0
#define PIPE 2
#define LOOK 2

__device__ __forceinline__ unsigned tree_sum(u32x2 own, const u32x2 (&x)[NSPLIT], const float (&wk)[NSPLIT]) {
#pragma clang fp contract(off)
  f32x2 t[NSPLIT];
  t[0] = __builtin_bit_cast(f32x2, own) * wk[0];
#pragma unroll
  for (int k = 1; k < NSPLIT; ++k) t[k] = __builtin_bit_cast(f32x2, x[k]) * wk[k];
  const f32x2 rr = ((t[0] + t[1]) + (t[2] + t[3])) + ((t[4] + t[5]) + (t[6] + t[7]));
  return pk(rr[0], rr[1]);
}
#define FINISH(X) (*(unsigned*)((char*)out + oo) = tree_sum(pv[0], X, wk))

template <int J> __device__ __forceinline__ void drain_rows(unsigned fa) {
  asm volatile("s_waitcnt vmcnt(%0)\n\tds_write_b32 %1, %2" :: "i"(4 * J), "v"(fa), "v"(NLT * 32 - 4 * J) : "memory");
  if constexpr (J > 0) drain_rows<J - 1>(fa);
}

__global__ void __launch_bounds__(NTHR, 2) __attribute__((aligned(8192)))
kv_split(const int* __restrict__ qk, const int* __restrict__ qv,
         const unsigned short* __restrict__ kzr, const unsigned short* __restrict__ ksc,
         const unsigned short* __restrict__ q,
         const unsigned short* __restrict__ vsc, const unsigned short* __restrict__ vzr,
         unsigned* out) {
  __shared__ __attribute__((aligned(16))) unsigned char smem[LDS_BYTES];
  const int tid = threadIdx.x, lane = tid & 63;
  const int wv = __builtin_amdgcn_readfirstlane(tid >> 6);
  const int bid = blockIdx.x;
  int gbase = 0;
#pragma unroll
  for (int k = 0; k < 8; ++k) {
    if (k > 0 && BB_[k] == BB_[k - 1] + 1) continue;
    const int n = (k < 7 && BB_[k + 1] == BB_[k] + 1) ? 3 : 1;
    gbase |= ((bid >> k) & n) << BB_[k];
  }
  const int voff = lane * 16;
  asm volatile("" : "+s"(gbase) : "s"(qk), "s"(qv), "s"(kzr), "s"(ksc), "s"(q), "s"(vsc), "s"(vzr), "s"(out));
  if (wv == 0) {
    __builtin_amdgcn_s_setprio(3);
    const unsigned lb = (unsigned)(size_t)(LAS unsigned char*)smem;
    const unsigned fa = lb + C_FULL, fr = lb + C_FREE;
    const int k7 = voff + ((unsigned)gbase >> 7), k8 = voff + ((unsigned)gbase >> 8);
#define PAR2(M0, VA, P, O0, O1) asm volatile("s_mov_b32 m0, %0\n\ts_nop 0\n\tglobal_load_lds_dwordx4 %1, %2 offset:" #O0 " nt\n\tglobal_load_lds_dwordx4 %1, %2 offset:" #O1 " nt" \
                                          :: "s"(lb + (M0)), "v"(VA), "s"(P) : "memory")
#define PAR1(M0, VA, P, POL) asm volatile("s_mov_b32 m0, %0\n\ts_nop 0\n\tglobal_load_lds_dwordx4 %1, %2" POL :: "s"(lb + (M0)), "v"(VA), "s"(P) : "memory")
    PAR2(KZ_OFF, k7, kzr, 0, 1024); PAR2(KS_OFF, k7, ksc, 0, 1024);
    PAR1(Q_OFF, voff + (gbase >> 22) * 1024, q, "");
    PAR2(KZ_OFF, k7 + 2048, kzr, 2048, 3072); PAR2(KS_OFF, k7 + 2048, ksc, 2048, 3072);
    PAR1(VS_OFF, k8, vsc, " nt"); PAR1(VZ_OFF, k8, vzr, " nt"); PAR1(VS_OFF + 1024, k8 + 2048, vsc, " nt"); PAR1(VZ_OFF + 1024, k8 + 2048, vzr, " nt");
    {
      const int xs = voff + (gbase >> 22) * XREC + X_SLOT + ((bid >> 3) & 7) * 2048;
      asm volatile("s_mov_b32 m0, %0\n\ts_nop 0\n\tglobal_load_lds_dwordx4 %1, %2 sc1\n\tglobal_load_lds_dwordx4 %1, %2 offset:1024 sc1" :: "s"(lb + OG_OFF), "v"(xs), "s"(xch) : "memory");
    }
    static_assert(pos2off(127) == 0x3f800 && SB_[2] < SB_[3] && SB_[3] < SB_[4] && SB_[4] < SB_[5] && SB_[5] < SB_[6], "address walk of the loader");
    const int vo1 = voff + pos2off(1) - 1024, vo2 = voff + pos2off(2) - 2048, vo3 = voff + pos2off(3) - 3072;
    unsigned mb = lb;
    int so = gbase, it = 0;
    int cnt = (4 - NFL) & ~3;
    unsigned long long src = (unsigned long long)qk;
    i32x4 f;
    asm volatile("" : "=v"(f));
#define ROW(J, V) "s_waitcnt vmcnt(%9)\n\tbuffer_load_dwordx4 " V ", %6, %7 offen offset:" #J "*1024 nt lds\n\t"
#pragma unroll 1
    do {
      const __amdgpu_buffer_rsrc_t rs = __builtin_amdgcn_make_buffer_rsrc((void*)src, 0, KV_BYTES, 0x00020000);
      asm volatile("s_mov_b32 m0, %1\n\t" ROW(0, "%2") ROW(1, "%3") ROW(2, "%4") ROW(3, "%5") "ds_write_b32 %8, %0\n\tv_add_u32 %0, 4, %0\n\ts_nop 4"
                   : "+v"(cnt) : "s"(mb), "v"(voff), "v"(vo1), "v"(vo2), "v"(vo3), "s"(rs), "s"(so), "v"(fa), "i"(NFL - 1) : "memory");
      ++it;
      mb += 4096;
      so = ((so | pos2off(3)) + pos2off(4)) & ~pos2off(3);
      if ((it & 7) == 7) asm volatile("ds_read_b128 %0, %1" : "=v"(f) : "v"(fr) : "memory");
      if ((it & 7) == 0) {
        const int T = it >> 3;
        if (T == 1) LDS_BARRIER();
        if (mb == lb + RING) mb = lb;
        if ((T & 3) == 0) { so = gbase | pos2off((T & 12) << 5); if (T == NT) src = (unsigned long long)qv; }
        asm volatile("s_waitcnt lgkmcnt(1)" : "+v"(f) :: "memory");
        while (T > 2 && T < NLT && __builtin_amdgcn_readfirstlane(min(min(f[0], f[1]), min(f[2], f[3]))) < T - 2) {
          __builtin_amdgcn_s_sleep(1);
          asm volatile("ds_read_b128 %0, %1\n\ts_waitcnt lgkmcnt(0)" : "=v"(f) : "v"(fr) : "memory");
        }
      }
    } while (it < NLT * 8);
    drain_rows<(NFL - 1) / 4>(fa);
    asm volatile("s_waitcnt lgkmcnt(0)\n\ts_endpgm");
  }
  const int w = wv - 1;
  const int lg = lane >> 4, c = lane & 15, r = lane & 31, h = lane >> 5;
  volatile LAS int* ctl = (volatile LAS int*)(smem + CTL);
  if (lane == 0) { ctl[w] = 0; ctl[4 + w] = 0; }
#define POST_STAGE(v) do { LDS_WAIT(); if (lane == 0) ctl[4 + w] = (v); } while (0)
#define WAIT_STAGE(v) do { while ((__builtin_amdgcn_readfirstlane(ctl[4] & ctl[5] & ctl[6] & ctl[7]) & (v)) != (v)) __builtin_amdgcn_s_sleep(1); asm volatile("" ::: "memory"); } while (0)
#define WAIT_ROWS(n) do { while (__builtin_amdgcn_readfirstlane(ctl[8]) < (n)) __builtin_amdgcn_s_sleep(1); asm volatile("" ::: "memory"); } while (0)
  static_assert(BB_[3] < 22 && BB_[4] < 22 && BB_[5] < 22, "blockIdx bits 3-5 pick the key split");
  const int split = (bid >> 3) & 7, bh = gbase >> 22;
  const __amdgpu_buffer_rsrc_t rsX = __builtin_amdgcn_make_buffer_rsrc((void*)(xch + bh * XREC), 0, XREC, 0x00020000);
  const int xo = X_SLOT + w * 512 + lane * 8;
  unsigned oldc = 0;
  if (tid < 64 + 5) oldc = __builtin_amdgcn_raw_buffer_load_b32(rsX, lane * 128, 0, SC1);
  if (TOUT && tid == 128) oldc = out[bh * 256 + split * 32];
  SB();
  LDS_BARRIER();
  unsigned char* kt = smem + PRIV + w * PRIV_W;
  const unsigned char* ringw = smem + w * 1024 + voff;
  unsigned char* kw = kt + (r >> 4) * KT_HALF + h * KT_RS + (r & 15) * 8;
  const unsigned char* kr = kt + (lg & 1) * KT_HALF + c * KT_RS + (lg >> 1) * 16;
  WAIT_ROWS(4);
  const u32x2 oldg = *(const u32x2*)(smem + OG_OFF + w * 512 + lane * 8);
  unsigned tag = (__builtin_amdgcn_readfirstlane(oldg[0]) + 1u) & 255u;
  while (__any(((oldg[0] & 255u) == tag) | ((oldg[1] & 255u) == tag))) tag = (tag + 1u) & 255u;
  const unsigned c0 = __builtin_amdgcn_readfirstlane(oldc);
  const u32x2 echo = {c0 ^ 0x9e3779b9u, c0 + 0x7f4a7c15u};
  if (lane == 0) {
    __builtin_amdgcn_raw_buffer_store_b32(tag, rsX, X_TAG + w * 32, split * 4, SC1);
    if (w == 0) __builtin_amdgcn_raw_buffer_store_b64(echo, rsX, X_ECHO, split * 8, SC1);
  }
  u32x4 Q[4];
#pragma unroll
  for (int s = 0; s < 4; ++s) Q[s] = *(const u32x4*)(smem + Q_OFF + (c >> 2) * (DH * 2) + 128 * (lg & 1) + 16 * (lg >> 1) + 32 * s);
  constexpr float SCALE = 0.088388347648318440f, LOG2E = 1.4426950408889634f;
  float mx = -INFINITY;
  int so = 0;

#pragma unroll 1
  for (int H = 0; H < 2 * NT; ++H) {
    const unsigned char* kp = smem + KZ_OFF + pos2g(H << 4) * 256 + r * 8;
    const u32x2 kzp = *(const u32x2*)kp, ksp = *(const u32x2*)(kp + 4096);
    WAIT_ROWS(H * 16 + 16);
    u32x4 x[4];
#pragma unroll
    for (int i = 0; i < 4; ++i) x[i] = *(const u32x4*)(ringw + so + i * 4096);
    LDS_WAIT();
    if (lane == 0) ctl[w] = (H + 1) >> 1;
    asm volatile("" : "+v"(x[0]), "+v"(x[1]), "+v"(x[2]), "+v"(x[3]) :: "memory");
    so = so == RING - SLOT / 2 ? 0 : so + SLOT / 2;
    const float kz0 = lo16(kzp[0]), kz1 = hi16(kzp[0]), kz2 = lo16(kzp[1]), kz3 = hi16(kzp[1]);
    const float ks0 = lo16(ksp[0]), ks1 = hi16(ksp[0]), ks2 = lo16(ksp[1]), ks3 = hi16(ksp[1]);
    unsigned char* kwh = kw + (H & 1) * 8 * KT_RS;
#pragma unroll
    for (int i = 0; i < 4; ++i) {
      const unsigned t01 = pk((float)(int)x[i][0] - kz0, (float)(int)x[i][1] - kz1), t23 = pk((float)(int)x[i][2] - kz2, (float)(int)x[i][3] - kz3);
      u32x2 kk;
      kk[0] = pk(lo16(t01) * ks0, hi16(t01) * ks1);
      kk[1] = pk(lo16(t23) * ks2, hi16(t23) * ks3);
      *(u32x2*)(kwh + i * 2 * KT_RS) = kk;
    }
    if (H & 1) {
      WAVE_FENCE();
      f32x4 d = {0.f, 0.f, 0.f, 0.f};
#pragma unroll
      for (int s = 0; s < 4; ++s) {
        const u32x4 bf = *(const u32x4*)(kr + s * 32);
        d = mfma(Q[s], bf, d);
      }
      WAVE_FENCE();
      const float sc = rbf(rbf(d[0]) * SCALE);
      mx = fmaxf(mx, sc);
      *(float*)(kt + SC_OFF + (H >> 1) * 256 + c * 16 + lg * 4) = sc;
    }
  }
  mx = fmaxf(mx, dpp<0x128>(mx)); mx = fmaxf(mx, dpp<0x124>(mx)); mx = fmaxf(mx, dpp<0x122>(mx)); mx = fmaxf(mx, dpp<0x121>(mx));
  if (c == 0) *(float*)(smem + C_MX + w * 16 + lg * 4) = mx;
  asm volatile("s_waitcnt vmcnt(0)" ::: "memory");
  POST_STAGE(1);
  WAIT_STAGE(1);
  f32x4 m = *(const f32x4*)(smem + C_MX);
#pragma unroll
  for (int ww = 1; ww < NCW; ++ww) {
    const f32x4 o = *(const f32x4*)(smem + C_MX + ww * 16);
#pragma unroll
    for (int j = 0; j < 4; ++j) m[j] = fmaxf(m[j], o[j]);
  }
  float lsum[4], cz[4];
  f32x4 acc[4];
#pragma unroll
  for (int j = 0; j < 4; ++j) { lsum[j] = 0.f; cz[j] = 0.f; acc[j] = f32x4{0.f, 0.f, 0.f, 0.f}; }
  const bool live = ((lane >> 3) & 1) == ((lane >> 4) & 1);
  const int lanev = pos2vp((lg << 5) | ((c >> 1) << 2) | w) + (c & 1) * 2;
  unsigned char* pt = kt + PT_OFF;
  const unsigned fulla = (unsigned)(size_t)(LAS unsigned char*)smem + C_FULL, ringa = (unsigned)(size_t)(LAS unsigned char*)smem + w * 1024 + voff;
  const __amdgpu_buffer_rsrc_t rsV = __builtin_amdgcn_make_buffer_rsrc((void*)qv, 0, KV_BYTES, 0x00020000);
  const int lastb = gbase | pos2off((NT - 1) << 5);
  const int lastv = pos2off(lg) + w * 128 + c * 8;
  u32x2 dr[16];
  auto v_tile = [&](const int T, auto peel_) {
    constexpr bool PEEL = decltype(peel_)::value;
    u32x4 A = *(const u32x4*)(pt + (T & 3) * 256 + h * 128 + (lane & 7) * 16);
    if (!live) A = u32x4{0u, 0u, 0u, 0u};
    u32x4 Bf[4];
#pragma unroll
    for (int mm = 0; mm < 4; ++mm) {
      u32x4 xa, xb;
      while (true) {
        int fv;
        asm volatile("ds_read_b32 %0, %3\n\tds_read_b128 %1, %4 offset:%5\n\tds_read_b128 %2, %4 offset:%6\n\ts_waitcnt lgkmcnt(0)"
                     : "=&v"(fv), "=&v"(xa), "=&v"(xb) : "v"(fulla), "v"(ringa + so), "i"(mm * 8192), "i"(mm * 8192 + 4096) : "memory");
        if (__builtin_amdgcn_readfirstlane(fv) >= (NT + T) * 32 + mm * 8 + 8) break;
        if (!PEEL) __builtin_amdgcn_s_sleep(2);
      }
      if (PEEL && (mm & 1) == 0) {
#pragma unroll
        for (int i = 4 * mm; i < 4 * mm + 8; ++i) { dr[i] = __builtin_amdgcn_raw_buffer_load_b64(rsV, lastv + (i & 1) * 512, lastb + pos2off((i >> 1) << 2), 2); SB(); }
      }
      if (mm == 3 && lane == 0) ctl[w] = NT + T + 1;
#pragma unroll
      for (int e = 0; e < 4; ++e) { unsigned t = pk((float)(int)xa[e], (float)(int)xb[e]); asm volatile("" : "+v"(t)); Bf[e][mm] = t; }
    }
    so = so == 2 * SLOT ? 0 : so + SLOT;
#pragma unroll
    for (int e = 0; e < 4; ++e) acc[e] = mfma(A, Bf[e], acc[e]);
    WAVE_FENCE();
  };
  u32x2 ev = __builtin_amdgcn_raw_buffer_load_b64(rsX, X_ECHO + (lane & 7) * 8, w == 0 ? 0 : OOB, SC1);
  unsigned tk = 0;
#pragma unroll 1
  for (int T = 0; T < NT - 2; ++T) {
    if ((T & 3) == 0) {
      const int tk2 = lanev ^ pos2vp(T << 5);
      const f32x4 S = *(const f32x4*)(kt + SC_OFF + T * 256 + voff);
      const float vs = lo16(*(const unsigned short*)(smem + VS_OFF + tk2));
      const float vz = lo16(*(const unsigned short*)(smem + VZ_OFF + tk2));
      unsigned char* pw = pt + lg * 256 + (c & 1) * 128 + (c >> 1) * 2;
#pragma unroll
      for (int j = 0; j < 4; ++j) {
        const float p = __builtin_amdgcn_exp2f((S[j] - m[j]) * LOG2E);
        const float pv = p * vs;
        const float hi = rbf(pv), lo = rbf(pv - hi);
        lsum[j] += p;
        cz[j] += (hi + lo) * vz;
        const int rt = (j & 1) + 4 * (j >> 1);
        *(unsigned short*)(pw + rt * 16) = (unsigned short)(__builtin_bit_cast(unsigned, hi) >> 16);
        *(unsigned short*)(pw + (rt + 2) * 16) = (unsigned short)(__builtin_bit_cast(unsigned, lo) >> 16);
      }
      if (T == NT - 4) {
        const float lv = wave_sum4(lsum), cv = wave_sum4(cz);
        if (c == 0) { *(float*)(smem + C_LC + w * 16 + lg * 4) = lv; *(float*)(smem + C_CZ + w * 16 + lg * 4) = cv; }
        POST_STAGE(3);
        if (w == 0) {
          WAIT_STAGE(3);
          if (lane < 4) {
            float L = 0.f, M = -INFINITY;
#pragma unroll
            for (int ww = 0; ww < NCW; ++ww) { L += *(const float*)(smem + C_LC + ww * 16 + lane * 4); M = fmaxf(M, *(const float*)(smem + C_MX + ww * 16 + lane * 4)); }
            __builtin_amdgcn_raw_buffer_store_b64(u32x2{__float_as_uint(M), __float_as_uint(L)}, rsX, X_ML + lane * 64, split * 8, SC1);
          }
        }
      }
      WAVE_FENCE();
    }
    v_tile(T, BoolC<false>{});
    if (T == NT - 4 && w == 0) {
#pragma unroll 1
      for (unsigned spin = 0; spin < SPIN_CAP && !__all((ev[0] == echo[0]) & (ev[1] == echo[1])); ++spin) {
        __builtin_amdgcn_s_sleep(8);
        ev = __builtin_amdgcn_raw_buffer_load_b64(rsX, X_ECHO + (lane & 7) * 8, 0, SC1);
      }
      asm volatile("s_waitcnt vmcnt(0)" ::: "memory");
      tk = 1u;
      if (lane == 0) asm volatile("buffer_atomic_add %0, off, %1, 0 sc0" : "+v"(tk) : "s"(rsX) : "memory");
    }
  }
  if (w == 0) {
    asm volatile("s_waitcnt vmcnt(0)" : "+v"(tk) :: "memory");
    if (lane == 0) ctl[9] = tk - c0;
    POST_STAGE(7);
  }
  while ((__builtin_amdgcn_readfirstlane(ctl[4]) & 4) == 0) __builtin_amdgcn_s_sleep(1);
  const bool desig = __builtin_amdgcn_readfirstlane(ctl[9]) == NSPLIT - 1;
  asm volatile("" ::: "memory");
  if (tid < 64 + 8) __builtin_amdgcn_raw_buffer_store_b64(u32x2{0u, 0u}, rsX, X_ECHO + lane * 8, desig ? 0 : OOB, SC1);
  const u32x2 mlv = __builtin_amdgcn_raw_buffer_load_b64(rsX, X_ML + lg * 64 + (c & 7) * 8, desig ? 0 : OOB, SC1);
  const unsigned tgv = __builtin_amdgcn_raw_buffer_load_b32(rsX, X_TAG + w * 32 + (c & 7) * 4, desig ? 0 : OOB, SC1);
  SB();
  v_tile(NT - 2, BoolC<true>{});
  SB();
  {
    f32x4 o0, o1;
#pragma unroll
    for (int e = 0; e < 4; ++e) { o0[e] = acc[e][0] + acc[e][2]; o1[e] = acc[e][1] + acc[e][3]; }
    float* ow = (float*)kt + 2 * (lg & 1) * DH + 64 * (lg >> 1) + 4 * c;
    *(f32x4*)ow = o0;
    *(f32x4*)(ow + DH) = o1;
  }
  LDS_BARRIER();
  f32x2 O = {0.f, 0.f};
#pragma unroll
  for (int ww = 0; ww < NCW; ++ww) O += *(const f32x2*)(smem + PRIV + ww * PRIV_W + (lg * DH + 32 * w + 2 * c) * 4) - *(const float*)(smem + C_CZ + ww * 16 + lg * 4);
  const unsigned char* pa = smem + PRIV + lg * PRIV_W + PT_OFF + 3 * 256 + (((c >> 2) & 1) + 2 * (c & 1) + 4 * (c >> 3)) * 16;
  const u32x4 A0 = *(const u32x4*)pa, A1 = *(const u32x4*)(pa + 128);
  f32x4 d0 = {O[0], 0.f, 0.f, 0.f}, d1 = {O[1], 0.f, 0.f, 0.f};
  int ofs[NSPLIT];
  u32x2 pv[NSPLIT];
#pragma unroll
  for (int k = 1; k < NSPLIT; ++k) { ofs[k] = desig ? (k ^ split) * 2048 : OOB; if (LOOK == 0) pv[k] = __builtin_amdgcn_raw_buffer_load_b64(rsX, xo, ofs[k], SC1); }
  SB();
  float wk[NSPLIT];
  unsigned tg[NSPLIT];
  {
    float M = __uint_as_float(mlv[0]);
    M = fmaxf(M, dpp<0xB1>(M)); M = fmaxf(M, dpp<0x4E>(M)); M = fmaxf(M, dpp<0x141>(M));
    float wt = __builtin_amdgcn_exp2f((__uint_as_float(mlv[0]) - M) * LOG2E);
    float L = __fmul_rn(wt, __uint_as_float(mlv[1]));
    L += dpp<0xB1>(L); L += dpp<0x4E>(L); L += dpp<0x141>(L);
    wt *= __builtin_amdgcn_rcpf(L);
#pragma unroll
    for (int k = 0; k < NSPLIT; ++k) { wk[k] = __shfl(wt, (lane & 48) | (k ^ split), 64); tg[k] = __builtin_amdgcn_readlane(tgv, k ^ split); asm volatile("" : "+v"(wk[k]), "+s"(tg[k])); }
  }
  unsigned oo = ((bh * 4 + lg) * 64 + 16 * w + c) * 4;
  asm volatile("" : "+v"(d0), "+v"(d1), "+v"(oo));
  SB();
  u32x4 B[2][2];
#pragma unroll
  for (int mm = 0; mm < 4; ++mm) {
#pragma unroll
    for (int ks = 0; ks < 2; ++ks)
#pragma unroll
      for (int e = 0; e < 2; ++e) B[ks][e][mm] = pk((float)(int)dr[4 * mm + ks][e], (float)(int)dr[4 * mm + 2 + ks][e]);
    SB();
    if (LOOK == mm + 1) {
#pragma unroll
      for (int k = 1; k < NSPLIT; ++k) pv[k] = __builtin_amdgcn_raw_buffer_load_b64(rsX, xo, ofs[k], SC1);
      SB();
    }
  }
  d0 = mfma(A0, B[0][0], d0); d1 = mfma(A0, B[0][1], d1);
  d0 = mfma(A1, B[1][0], d0); d1 = mfma(A1, B[1][1], d1);
  pv[0] = u32x2{__float_as_uint(d0[0] + d0[1]) & ~255u, __float_as_uint(d1[0] + d1[1]) & ~255u};
  __builtin_amdgcn_raw_buffer_store_b64(u32x2{pv[0][0] | tag, pv[0][1] | tag}, rsX, xo, desig ? OOB : split * 2048, SC1);
  if (!desig) asm volatile("s_endpgm");
  SB();
  u32x2 nx[NSPLIT];
#pragma unroll 1
  for (unsigned spin = 0; ; ++spin) {
#pragma unroll
    for (int k = 1; k < NSPLIT; ++k) nx[k] = __builtin_amdgcn_raw_buffer_load_b64(rsX, xo, ofs[k], SC1);
    SB();
    unsigned bad = 0;
#pragma unroll
    for (int k = 1; k < NSPLIT; ++k) { pv[k][0] ^= tg[k]; pv[k][1] ^= tg[k]; bad |= pv[k][0] | pv[k][1]; }
    if (__builtin_amdgcn_ballot_w64((bad & 255u) != 0) == 0 || spin == SPIN_CAP) break;
    asm volatile("" ::: "memory");
#pragma unroll
    for (int k = 1; k < NSPLIT; ++k) pv[k] = nx[k];
  }
  FINISH(pv);
}
}

extern "C" void kernel_launch(void* const* d_in, const int* in_sizes, int n_in, void* d_out, int out_size,
                              void* d_ws, size_t ws_size, hipStream_t stream) {
  const unsigned short* q = (const unsigned short*)d_in[0];
  const int* qk = (const int*)d_in[1];
  const int* qv = (const int*)d_in[2];
  const unsigned short* ks = (const unsigned short*)d_in[3];
  const unsigned short* kz = (const unsigned short*)d_in[4];
  const unsigned short* vs = (const unsigned short*)d_in[5];
  const unsigned short* vz = (const unsigned short*)d_in[6];
  kv_split<<<NBH * NSPLIT, NTHR, 0, stream>>>(qk, qv, kz, ks, q, vs, vz, (unsigned*)d_out);
}
```

```cpp
#include <hip/hip_runtime.h>
#include <hip/hip_bf16.h>
#include <stdint.h>

namespace {
constexpr int NBATCH = 4, HQ = 32, HKV = 8, SKV = 8192, DH = 128;
constexpr int NBH = NBATCH * HKV;
constexpr int NSPLIT = 8;
constexpr int NCW = 4;
constexpr int NT = 16;
constexpr int NLT = 2 * NT - 1;
constexpr int NTHR = (NCW + 1) * 64;
constexpr int KV_BYTES = NBH * SKV * DH * 4;
constexpr int NFL = 22;

constexpr int SB_[9] = {13, 14, 11, 12, 15, 16, 17, 10, 19};
constexpr int BB_[8] = {23, 22, 25, 18, 20, 21, 24, 26};
__host__ __device__ constexpr int pos2off(int x) { int o = 0; for (int k = 0; k < 9; ++k) o |= ((x >> k) & 1) << SB_[k]; return o; }
__host__ __device__ constexpr int pos2g(int x) { return ((pos2off(x) >> 15) & 7) | ((pos2off(x) >> 16) & 8); }
__host__ __device__ constexpr int pos2vp(int x) { return pos2g(x) * 128 + ((pos2off(x) >> 8) & 0x7c); }
static_assert(pos2off(511) == 0xbfc00, "a workgroup covers 16 whole groups");
static_assert(pos2g(15) == 0, "four consecutive rows of a consumer lie in one group");
static_assert(BB_[3] < 22 && BB_[4] < 22 && BB_[5] < 22, "blockIdx bits 3-5 pick the key split");

typedef unsigned u32x4 __attribute__((ext_vector_type(4)));
typedef unsigned u32x2 __attribute__((ext_vector_type(2)));
typedef int i32x4 __attribute__((ext_vector_type(4)));
typedef float f32x4 __attribute__((ext_vector_type(4)));
typedef float f32x2 __attribute__((ext_vector_type(2)));
typedef __bf16 bf16x2 __attribute__((ext_vector_type(2)));
typedef __bf16 bf16x8 __attribute__((ext_vector_type(8)));
template <bool B> struct BoolC { static constexpr bool value = B; };
#define LAS __attribute__((address_space(3)))

__device__ __forceinline__ unsigned pk(float lo, float hi) {
  bf16x2 v = {(__bf16)lo, (__bf16)hi};
  return __builtin_bit_cast(unsigned, v);
}
__device__ __forceinline__ float lo16(unsigned u) { return __builtin_bit_cast(float, u << 16); }
__device__ __forceinline__ float hi16(unsigned u) { return __builtin_bit_cast(float, u & 0xffff0000u); }
__device__ __forceinline__ float rbf(float x) { return (float)(__bf16)x; }
__device__ __forceinline__ f32x4 mfma(u32x4 a, u32x4 b, f32x4 c) {
  return __builtin_amdgcn_mfma_f32_16x16x32_bf16(__builtin_bit_cast(bf16x8, a), __builtin_bit_cast(bf16x8, b), c, 0, 0, 0);
}
template <int CTRL> __device__ __forceinline__ float dpp(float x) {
  return __builtin_bit_cast(float, __builtin_amdgcn_mov_dpp(__builtin_bit_cast(int, x), CTRL, 0xf, 0xf, true));
}
__device__ __forceinline__ float wave_sum4(const float (&x)[4]) {
  auto s = __builtin_amdgcn_permlane32_swap(__float_as_uint(x[0]), __float_as_uint(x[2]), false, false);
  auto t = __builtin_amdgcn_permlane32_swap(__float_as_uint(x[1]), __float_as_uint(x[3]), false, false);
  auto u = __builtin_amdgcn_permlane16_swap(__float_as_uint(__uint_as_float(s[0]) + __uint_as_float(s[1])),
                                            __float_as_uint(__uint_as_float(t[0]) + __uint_as_float(t[1])), false, false);
  float y = __uint_as_float(u[0]) + __uint_as_float(u[1]);
  y += dpp<0x128>(y); y += dpp<0x124>(y); y += dpp<0x122>(y); y += dpp<0x121>(y);
  return y;
}
#define WAVE_FENCE() do { __builtin_amdgcn_fence(__ATOMIC_ACQ_REL, "wavefront"); __builtin_amdgcn_wave_barrier(); } while (0)
#define SB() __builtin_amdgcn_sched_barrier(0)
#define LDS_WAIT() asm volatile("s_waitcnt lgkmcnt(0)" ::: "memory")
#define LDS_BARRIER() asm volatile("s_waitcnt lgkmcnt(0)\n\ts_barrier" ::: "memory")

constexpr int SLOT = 32 * 1024, RING = 3 * SLOT;
constexpr int KZ_OFF = RING, KS_OFF = KZ_OFF + 4096;
constexpr int VS_OFF = KS_OFF + 4096;
constexpr int VZ_OFF = VS_OFF + 2048;
constexpr int Q_OFF = VZ_OFF + 2048;
constexpr int OG_OFF = Q_OFF + 1024;
constexpr int PRIV = OG_OFF + 2048;
constexpr int KT_RS = 144, KT_HALF = 16 * KT_RS, KT_BYTES = 2 * KT_HALF;
constexpr int PT_OFF = KT_BYTES;
constexpr int SC_OFF = PT_OFF + 1024;
constexpr int PRIV_W = SC_OFF + 4096;
constexpr int CTL = PRIV + NCW * PRIV_W;
constexpr int C_FREE = CTL, C_FULL = CTL + 32, C_MX = CTL + 48, C_LC = C_MX + NCW * 16, C_CZ = C_LC + NCW * 16, C_TG = C_CZ + NCW * 16;
constexpr int LDS_BYTES = C_TG + 16;
static_assert(LDS_BYTES <= 160 * 1024, "LDS");

constexpr int X_ECHO = 128, X_REC = 256, X_SLOT = 1024, XREC = X_SLOT + NSPLIT * 2048;
constexpr int OOB = 0x40000000, SC1 = 16;
constexpr unsigned SPIN_CAP = 1u << 18;

__global__ void __launch_bounds__(NTHR, 2) __attribute__((aligned(8192)))
kv_decode(const int* __restrict__ qk, const int* __restrict__ qv,
          const unsigned short* __restrict__ kzr, const unsigned short* __restrict__ ksc,
          const unsigned short* __restrict__ q,
          const unsigned short* __restrict__ vsc, const unsigned short* __restrict__ vzr,
          unsigned char* ws, unsigned* out) {
  __shared__ __attribute__((aligned(16))) unsigned char smem[LDS_BYTES];
  const int tid = threadIdx.x, lane = tid & 63;
  const int wv = __builtin_amdgcn_readfirstlane(tid >> 6);
  const int bid = blockIdx.x;
  int gbase = 0;
#pragma unroll
  for (int k = 0; k < 8; ++k) {
    if (k > 0 && BB_[k] == BB_[k - 1] + 1) continue;
    const int n = (k < 7 && BB_[k + 1] == BB_[k] + 1) ? 3 : 1;
    gbase |= ((bid >> k) & n) << BB_[k];
  }
  const int voff = lane * 16;
  asm volatile("" : "+s"(gbase) : "s"(qk), "s"(qv), "s"(kzr), "s"(ksc), "s"(q), "s"(vsc), "s"(vzr), "s"(ws), "s"(out));
  const int split = (bid >> 3) & 7, bh = gbase >> 22;
  if (wv == 0) {
    __builtin_amdgcn_s_setprio(3);
    const unsigned lb = (unsigned)(size_t)(LAS unsigned char*)smem;
    const unsigned fa = lb + C_FULL, fr = lb + C_FREE;
    const int k7 = voff + ((unsigned)gbase >> 7), k8 = voff + ((unsigned)gbase >> 8);
#define PAR2(M0, VA, P, O0, O1, POL) asm volatile("s_mov_b32 m0, %0\n\ts_nop 0\n\tglobal_load_lds_dwordx4 %1, %2 offset:" #O0 POL "\n\tglobal_load_lds_dwordx4 %1, %2 offset:" #O1 POL \
                                               :: "s"(lb + (M0)), "v"(VA), "s"(P) : "memory")
#define PAR1(M0, VA, P, POL) asm volatile("s_mov_b32 m0, %0\n\ts_nop 0\n\tglobal_load_lds_dwordx4 %1, %2" POL :: "s"(lb + (M0)), "v"(VA), "s"(P) : "memory")
    PAR2(KZ_OFF, k7, kzr, 0, 1024, " nt"); PAR2(KS_OFF, k7, ksc, 0, 1024, " nt");
    PAR1(Q_OFF, voff + bh * 1024, q, "");
    PAR2(KZ_OFF, k7 + 2048, kzr, 2048, 3072, " nt"); PAR2(KS_OFF, k7 + 2048, ksc, 2048, 3072, " nt");
    PAR1(VS_OFF, k8, vsc, " nt"); PAR1(VZ_OFF, k8, vzr, " nt"); PAR1(VS_OFF + 1024, k8 + 2048, vsc, " nt"); PAR1(VZ_OFF + 1024, k8 + 2048, vzr, " nt");
    PAR2(OG_OFF, voff + bh * XREC + X_SLOT + split * 2048, ws, 0, 1024, " sc1");
    static_assert(pos2off(127) == 0x3f800 && SB_[2] < SB_[3] && SB_[3] < SB_[4] && SB_[4] < SB_[5] && SB_[5] < SB_[6], "address walk of the loader");
    const int vo1 = voff + pos2off(1) - 1024, vo2 = voff + pos2off(2) - 2048, vo3 = voff + pos2off(3) - 3072;
    unsigned mb = lb;
    int so = gbase, it = 0;
    int cnt = (4 - NFL) & ~3;
    unsigned long long src = (unsigned long long)qk;
    i32x4 f;
    asm volatile("" : "=v"(f));
#define ROW(J, V) "s_waitcnt vmcnt(%9)\n\tbuffer_load_dwordx4 " V ", %6, %7 offen offset:" #J "*1024 nt lds\n\t"
#pragma unroll 1
    do {
      const __amdgpu_buffer_rsrc_t rs = __builtin_amdgcn_make_buffer_rsrc((void*)src, 0, KV_BYTES, 0x00020000);
      asm volatile("s_mov_b32 m0, %1\n\t" ROW(0, "%2") ROW(1, "%3") ROW(2, "%4") ROW(3, "%5") "ds_write_b32 %8, %0\n\tv_add_u32 %0, 4, %0\n\ts_nop 4"
                   : "+v"(cnt) : "s"(mb), "v"(voff), "v"(vo1), "v"(vo2), "v"(vo3), "s"(rs), "s"(so), "v"(fa), "i"(NFL - 1) : "memory");
      ++it;
      mb += 4096;
      so = ((so | pos2off(3)) + pos2off(4)) & ~pos2off(3);
      if ((it & 7) == 7) asm volatile("ds_read_b128 %0, %1" : "=v"(f) : "v"(fr) : "memory");
      if ((it & 7) == 0) {
        const int T = it >> 3;
        if (T == 1) LDS_BARRIER();
        if (mb == lb + RING) mb = lb;
        if ((T & 3) == 0) { so = gbase | pos2off((T & 12) << 5); if (T == NT) src = (unsigned long long)qv; }
        asm volatile("s_waitcnt lgkmcnt(1)" : "+v"(f) :: "memory");
        while (T > 2 && T < NLT && __builtin_amdgcn_readfirstlane(min(min(f[0], f[1]), min(f[2], f[3]))) < T - 2) {
          __builtin_amdgcn_s_sleep(1);
          asm volatile("ds_read_b128 %0, %1\n\ts_waitcnt lgkmcnt(0)" : "=v"(f) : "v"(fr) : "memory");
        }
      }
    } while (it < NLT * 8);
    static_assert(((4 - NFL) & ~3) == -20, "the drain starts 20 rows before the end");
#define DRAIN(N) "s_waitcnt vmcnt(" #N ")\n\tds_write_b32 %1, %0\n\tv_add_u32 %0, 4, %0\n\t"
    asm volatile(DRAIN(20) DRAIN(16) DRAIN(12) DRAIN(8) DRAIN(4) DRAIN(0) "s_waitcnt lgkmcnt(0)\n\ts_endpgm" : "+v"(cnt) : "v"(fa) : "memory");
  }
  const int w = wv - 1;
  const int lg = lane >> 4, c = lane & 15, r = lane & 31, h = lane >> 5;
  volatile LAS int* ctl = (volatile LAS int*)(smem + CTL);
  if (lane == 0) { ctl[w] = 0; ctl[4 + w] = 0; }
#define POST_STAGE(v) do { LDS_WAIT(); if (lane == 0) ctl[4 + w] = (v); } while (0)
#define WAIT_STAGE(v) do { while (__builtin_amdgcn_ballot_w64(ctl[4 + (lane & 3)] < (v)) != 0) __builtin_amdgcn_s_sleep(1); asm volatile("" ::: "memory"); } while (0)
#define WAIT_ROWS(n) do { while (__builtin_amdgcn_readfirstlane(ctl[8]) < (n)) __builtin_amdgcn_s_sleep(1); asm volatile("" ::: "memory"); } while (0)
  const __amdgpu_buffer_rsrc_t rsX = __builtin_amdgcn_make_buffer_rsrc((void*)(ws + bh * XREC), 0, XREC, 0x00020000);
  const int xo = X_SLOT + w * 512 + lane * 8;
  unsigned oldc = 0;
  if (tid < 64 + 6) asm volatile("buffer_load_dword %0, %1, %2, 0 offen sc1" : "+v"(oldc) : "v"(lane * 128), "s"(rsX) : "memory");
  SB();
  LDS_BARRIER();
  unsigned char* kt = smem + PRIV + w * PRIV_W;
  const unsigned char* ringw = smem + w * 1024 + voff;
  unsigned char* kw = kt + (r >> 4) * KT_HALF + h * KT_RS + (r & 15) * 8;
  const unsigned char* kr = kt + (lg & 1) * KT_HALF + c * KT_RS + (lg >> 1) * 16;
  WAIT_ROWS(4);
  const u32x2 oldg = *(const u32x2*)(smem + OG_OFF + w * 512 + lane * 8);
  unsigned tag = (__builtin_amdgcn_readfirstlane(oldg[0]) + 1u) & 255u;
  while (__any(((oldg[0] & 255u) == tag) | ((oldg[1] & 255u) == tag))) tag = (tag + 1u) & 255u;
  asm volatile("s_waitcnt vmcnt(0)" : "+v"(oldc) :: "memory");
  const unsigned c0 = __builtin_amdgcn_readfirstlane(oldc);
  const u32x2 echo = {c0 ^ 0x9e3779b9u, c0 + 0x7f4a7c15u};
  if (lane == 0) {
    *(unsigned*)(smem + C_TG + w * 4) = tag;
    if (w == 0) __builtin_amdgcn_raw_buffer_store_b64(echo, rsX, X_ECHO, split * 8, SC1);
  }
  u32x4 Q[4];
#pragma unroll
  for (int s = 0; s < 4; ++s) Q[s] = *(const u32x4*)(smem + Q_OFF + (c >> 2) * (DH * 2) + 128 * (lg & 1) + 16 * (lg >> 1) + 32 * s);
  constexpr float SCALE = 0.088388347648318440f, LOG2E = 1.4426950408889634f;
  float mx = -INFINITY;
  int so = 0;

#pragma unroll 1
  for (int H = 0; H < 2 * NT; ++H) {
    const unsigned char* kp = smem + KZ_OFF + pos2g(H << 4) * 256 + r * 8;
    const u32x2 kzp = *(const u32x2*)kp, ksp = *(const u32x2*)(kp + 4096);
    WAIT_ROWS(H * 16 + 16);
    u32x4 x[4];
#pragma unroll
    for (int i = 0; i < 4; ++i) x[i] = *(const u32x4*)(ringw + so + i * 4096);
    LDS_WAIT();
    if (lane == 0) ctl[w] = (H + 1) >> 1;
    asm volatile("" : "+v"(x[0]), "+v"(x[1]), "+v"(x[2]), "+v"(x[3]) :: "memory");
    so = so == RING - SLOT / 2 ? 0 : so + SLOT / 2;
    const float kz0 = lo16(kzp[0]), kz1 = hi16(kzp[0]), kz2 = lo16(kzp[1]), kz3 = hi16(kzp[1]);
    const float ks0 = lo16(ksp[0]), ks1 = hi16(ksp[0]), ks2 = lo16(ksp[1]), ks3 = hi16(ksp[1]);
    unsigned char* kwh = kw + (H & 1) * 8 * KT_RS;
#pragma unroll
    for (int i = 0; i < 4; ++i) {
      const unsigned t01 = pk((float)(int)x[i][0] - kz0, (float)(int)x[i][1] - kz1), t23 = pk((float)(int)x[i][2] - kz2, (float)(int)x[i][3] - kz3);
      u32x2 kk;
      kk[0] = pk(lo16(t01) * ks0, hi16(t01) * ks1);
      kk[1] = pk(lo16(t23) * ks2, hi16(t23) * ks3);
      *(u32x2*)(kwh + i * 2 * KT_RS) = kk;
    }
    if (H & 1) {
      WAVE_FENCE();
      f32x4 d = {0.f, 0.f, 0.f, 0.f};
#pragma unroll
      for (int s = 0; s < 4; ++s) {
        const u32x4 bf = *(const u32x4*)(kr + s * 32);
        d = mfma(Q[s], bf, d);
      }
      WAVE_FENCE();
      const float sc = rbf(rbf(d[0]) * SCALE);
      mx = fmaxf(mx, sc);
      *(float*)(kt + SC_OFF + (H >> 1) * 256 + c * 16 + lg * 4) = sc;
    }
  }
  mx = fmaxf(mx, dpp<0x128>(mx)); mx = fmaxf(mx, dpp<0x124>(mx)); mx = fmaxf(mx, dpp<0x122>(mx)); mx = fmaxf(mx, dpp<0x121>(mx));
  if (c == 0) *(float*)(smem + C_MX + w * 16 + lg * 4) = mx;
  POST_STAGE(1);
  WAIT_STAGE(1);
  f32x4 m = *(const f32x4*)(smem + C_MX);
#pragma unroll
  for (int ww = 1; ww < NCW; ++ww) {
    const f32x4 o = *(const f32x4*)(smem + C_MX + ww * 16);
#pragma unroll
    for (int j = 0; j < 4; ++j) m[j] = fmaxf(m[j], o[j]);
  }
  float lsum[4], cz[4];
  f32x4 acc[4];
#pragma unroll
  for (int j = 0; j < 4; ++j) { lsum[j] = 0.f; cz[j] = 0.f; acc[j] = f32x4{0.f, 0.f, 0.f, 0.f}; }
  const bool live = ((lane >> 3) & 1) == ((lane >> 4) & 1);
  const int lanev = pos2vp((lg << 5) | ((c >> 1) << 2) | w) + (c & 1) * 2;
  unsigned char* pt = kt + PT_OFF;
  const unsigned fulla = (unsigned)(size_t)(LAS unsigned char*)smem + C_FULL, ringa = (unsigned)(size_t)(LAS unsigned char*)smem + w * 1024 + voff;
  const __amdgpu_buffer_rsrc_t rsV = __builtin_amdgcn_make_buffer_rsrc((void*)qv, 0, KV_BYTES, 0x00020000);
  const int lastb = gbase | pos2off((NT - 1) << 5);
  const int lastv = pos2off(lg) + w * 128 + c * 8;
  u32x2 dr[16];
  u32x2 ev = __builtin_amdgcn_raw_buffer_load_b64(rsX, X_ECHO + (lane & 7) * 8, w == 0 ? 0 : OOB, SC1);
  unsigned tk = 0;
  auto v_tile = [&](const int T, auto peel_) {
    constexpr bool PEEL = decltype(peel_)::value;
    u32x4 A = *(const u32x4*)(pt + (T & 3) * 256 + h * 128 + (lane & 7) * 16);
    if (!live) A = u32x4{0u, 0u, 0u, 0u};
    u32x4 Bf[4];
#pragma unroll
    for (int mm = 0; mm < 4; ++mm) {
      u32x4 xa, xb;
      while (true) {
        int fv;
        asm volatile("ds_read_b32 %0, %3\n\tds_read_b128 %1, %4 offset:%5\n\tds_read_b128 %2, %4 offset:%6\n\ts_waitcnt lgkmcnt(0)"
                     : "=&v"(fv), "=&v"(xa), "=&v"(xb) : "v"(fulla), "v"(ringa + so), "i"(mm * 8192), "i"(mm * 8192 + 4096) : "memory");
        if (__builtin_amdgcn_readfirstlane(fv) >= (NT + T) * 32 + mm * 8 + 8) break;
        if (!PEEL) __builtin_amdgcn_s_sleep(2);
      }
      if (PEEL && (mm & 1) == 0) {
#pragma unroll
        for (int i = 4 * mm; i < 4 * mm + 8; ++i) { dr[i] = __builtin_amdgcn_raw_buffer_load_b64(rsV, lastv + (i & 1) * 512, lastb + pos2off((i >> 1) << 2), 2); SB(); }
      }
      if (mm == 3 && lane == 0) ctl[w] = NT + T + 1;
#pragma unroll
      for (int e = 0; e < 4; ++e) { unsigned t = pk((float)(int)xa[e], (float)(int)xb[e]); asm volatile("" : "+v"(t)); Bf[e][mm] = t; }
    }
    so = so == 2 * SLOT ? 0 : so + SLOT;
#pragma unroll
    for (int e = 0; e < 4; ++e) acc[e] = mfma(A, Bf[e], acc[e]);
    WAVE_FENCE();
  };
#pragma unroll 1
  for (int T = 0; T < NT - 2; ++T) {
    if ((T & 3) == 0) {
      const int tk2 = lanev ^ pos2vp(T << 5);
      const f32x4 S = *(const f32x4*)(kt + SC_OFF + T * 256 + voff);
      const float vs = lo16(*(const unsigned short*)(smem + VS_OFF + tk2));
      const float vz = lo16(*(const unsigned short*)(smem + VZ_OFF + tk2));
      unsigned char* pw = pt + lg * 256 + (c & 1) * 128 + (c >> 1) * 2;
#pragma unroll
      for (int j = 0; j < 4; ++j) {
        const float p = __builtin_amdgcn_exp2f((S[j] - m[j]) * LOG2E);
        const float pv = p * vs;
        const float hi = rbf(pv), lo = rbf(pv - hi);
        lsum[j] += p;
        cz[j] += (hi + lo) * vz;
        const int rt = (j & 1) + 4 * (j >> 1);
        *(unsigned short*)(pw + rt * 16) = (unsigned short)(__builtin_bit_cast(unsigned, hi) >> 16);
        *(unsigned short*)(pw + (rt + 2) * 16) = (unsigned short)(__builtin_bit_cast(unsigned, lo) >> 16);
      }
      if (T == NT - 4) {
        const float lv = wave_sum4(lsum), cv = wave_sum4(cz);
        if (c == 0) { *(float*)(smem + C_LC + w * 16 + lg * 4) = lv; *(float*)(smem + C_CZ + w * 16 + lg * 4) = cv; }
        POST_STAGE(2);
        if (w == 0) {
          WAIT_STAGE(2);
          if (lane < 4) {
            float L = 0.f;
#pragma unroll
            for (int ww = 0; ww < NCW; ++ww) L += *(const float*)(smem + C_LC + ww * 16 + lane * 4);
            const float Ma = (lane & 1) ? m[1] : m[0], Mb = (lane & 1) ? m[3] : m[2], M = (lane & 2) ? Mb : Ma;
            __builtin_amdgcn_raw_buffer_store_b128(u32x4{__float_as_uint(M), __float_as_uint(L), *(const unsigned*)(smem + C_TG + lane * 4), 0u},
                                                   rsX, X_REC + lane * 16, split * 64, SC1);
          }
        }
      }
      WAVE_FENCE();
    }
    v_tile(T, BoolC<false>{});
    if (T == NT - 3 && w == 0) {
#pragma unroll 1
      for (unsigned spin = 0; spin < SPIN_CAP && !__all((ev[0] == echo[0]) & (ev[1] == echo[1])); ++spin) {
        __builtin_amdgcn_s_sleep(8);
        ev = __builtin_amdgcn_raw_buffer_load_b64(rsX, X_ECHO + (lane & 7) * 8, 0, SC1);
      }
      asm volatile("s_waitcnt vmcnt(0)" ::: "memory");
      tk = 1u;
      if (lane == 0) asm volatile("buffer_atomic_add %0, off, %1, 0 sc0" : "+v"(tk) : "s"(rsX) : "memory");
    }
  }
  v_tile(NT - 2, BoolC<true>{});
  SB();
  {
    f32x4 o0, o1;
#pragma unroll
    for (int e = 0; e < 4; ++e) { o0[e] = acc[e][0] + acc[e][2]; o1[e] = acc[e][1] + acc[e][3]; }
    float* ow = (float*)kt + 2 * (lg & 1) * DH + 64 * (lg >> 1) + 4 * c;
    *(f32x4*)ow = o0;
    *(f32x4*)(ow + DH) = o1;
  }
  if (w == 0) {
    asm volatile("s_waitcnt vmcnt(16)" : "+v"(tk) :: "memory");
    if (lane == 0) ctl[9] = tk - c0;
  }
  LDS_BARRIER();
  const bool last = __builtin_amdgcn_readfirstlane(ctl[9]) == NSPLIT - 1;
  if (last && tid < 64 + 8) __builtin_amdgcn_raw_buffer_store_b64(u32x2{0u, 0u}, rsX, X_ECHO + lane * 8, 0, SC1);
  const u32x2 mlv = __builtin_amdgcn_raw_buffer_load_b64(rsX, X_REC + lg * 16 + (c & 7) * 64, last ? 0 : OOB, SC1);
  const unsigned tgv = __builtin_amdgcn_raw_buffer_load_b32(rsX, X_REC + 8 + w * 16 + (c & 7) * 64, last ? 0 : OOB, SC1);
  SB();
  f32x2 O = {0.f, 0.f};
#pragma unroll
  for (int ww = 0; ww < NCW; ++ww)
    O += *(const f32x2*)(smem + PRIV + ww * PRIV_W + (lg * DH + 32 * w + 2 * c) * 4) - *(const float*)(smem + C_CZ + ww * 16 + lg * 4);
  const unsigned char* pa = smem + PRIV + lg * PRIV_W + PT_OFF + 3 * 256 + (((c >> 2) & 1) + 2 * (c & 1) + 4 * (c >> 3)) * 16;
  const u32x4 A0 = *(const u32x4*)pa, A1 = *(const u32x4*)(pa + 128);
  f32x4 d0 = {O[0], 0.f, 0.f, 0.f}, d1 = {O[1], 0.f, 0.f, 0.f};
  int ofs[NSPLIT];
  u32x2 pv[NSPLIT];
#pragma unroll
  for (int k = 1; k < NSPLIT; ++k) ofs[k] = last ? (k ^ split) * 2048 : OOB;
  SB();
  unsigned oo = ((bh * 4 + lg) * 64 + 16 * w + c) * 4;
  asm volatile("" : "+v"(d0), "+v"(d1), "+v"(oo));
  SB();
  u32x4 B[2][2];
#pragma unroll
  for (int mm = 0; mm < 4; ++mm) {
#pragma unroll
    for (int ks = 0; ks < 2; ++ks)
#pragma unroll
      for (int e = 0; e < 2; ++e) B[ks][e][mm] = pk((float)(int)dr[4 * mm + ks][e], (float)(int)dr[4 * mm + 2 + ks][e]);
    SB();
    if (mm == 1) {
#pragma unroll
      for (int k = 1; k < NSPLIT; ++k) pv[k] = __builtin_amdgcn_raw_buffer_load_b64(rsX, xo, ofs[k], SC1);
      SB();
    }
  }
  d0 = mfma(A0, B[0][0], d0); d1 = mfma(A0, B[0][1], d1);
  d0 = mfma(A1, B[1][0], d0); d1 = mfma(A1, B[1][1], d1);
  pv[0] = u32x2{__float_as_uint(d0[0] + d0[1]) & ~255u, __float_as_uint(d1[0] + d1[1]) & ~255u};
  if (!last) {
    __builtin_amdgcn_raw_buffer_store_b64(u32x2{pv[0][0] | tag, pv[0][1] | tag}, rsX, xo, split * 2048, SC1);
    asm volatile("s_endpgm" ::: "memory");
  }
  SB();
  float wk[NSPLIT];
  unsigned tg[NSPLIT];
  {
    float M = __uint_as_float(mlv[0]);
    M = fmaxf(M, dpp<0xB1>(M)); M = fmaxf(M, dpp<0x4E>(M)); M = fmaxf(M, dpp<0x141>(M));
    float wt = __builtin_amdgcn_exp2f((__uint_as_float(mlv[0]) - M) * LOG2E);
    float L = __fmul_rn(wt, __uint_as_float(mlv[1]));
    L += dpp<0xB1>(L); L += dpp<0x4E>(L); L += dpp<0x141>(L);
    wt *= __builtin_amdgcn_rcpf(L);
#pragma unroll
    for (int k = 0; k < NSPLIT; ++k) {
      wk[k] = __shfl(wt, (lane & 48) | (k ^ split), 64);
      tg[k] = __builtin_amdgcn_readlane(tgv, k ^ split);
      asm volatile("" : "+v"(wk[k]), "+s"(tg[k]));
    }
  }
  u32x2 nx[NSPLIT];
#pragma unroll 1
  for (unsigned spin = 0; ; ++spin) {
#pragma unroll
    for (int k = 1; k < NSPLIT; ++k) nx[k] = __builtin_amdgcn_raw_buffer_load_b64(rsX, xo, ofs[k], SC1);
    SB();
    unsigned bad = 0;
#pragma unroll
    for (int k = 1; k < NSPLIT; ++k) { pv[k][0] ^= tg[k]; pv[k][1] ^= tg[k]; bad |= pv[k][0] | pv[k][1]; }
    if (__builtin_amdgcn_ballot_w64((bad & 255u) != 0) == 0 || spin == SPIN_CAP) break;
    asm volatile("" ::: "memory");
#pragma unroll
    for (int k = 1; k < NSPLIT; ++k) pv[k] = nx[k];
  }
  {
#pragma clang fp contract(off)
    f32x2 t[NSPLIT];
#pragma unroll
    for (int k = 0; k < NSPLIT; ++k) { const u32x2 xk = pv[k]; t[k] = __builtin_bit_cast(f32x2, xk) * wk[k]; }
    const f32x2 rr = ((t[0] + t[1]) + (t[2] + t[3])) + ((t[4] + t[5]) + (t[6] + t[7]));
    *(unsigned*)((char*)out + oo) = pk(rr[0], rr[1]);
  }
}
}

extern "C" void kernel_launch(void* const* d_in, const int* in_sizes, int n_in, void* d_out, int out_size,
                              void* d_ws, size_t ws_size, hipStream_t stream) {
  const unsigned short* q = (const unsigned short*)d_in[0];
  const int* qk = (const int*)d_in[1];
  const int* qv = (const int*)d_in[2];
  const unsigned short* ks = (const unsigned short*)d_in[3];
  const unsigned short* kz = (const unsigned short*)d_in[4];
  const unsigned short* vs = (const unsigned short*)d_in[5];
  const unsigned short* vz = (const unsigned short*)d_in[6];
  kv_decode<<<NBH * NSPLIT, NTHR, 0, stream>>>(qk, qv, kz, ks, q, vs, vz, (unsigned char*)d_ws, (unsigned*)d_out);
}
```

```cpp
#include <hip/hip_runtime.h>
#include <hip/hip_bf16.h>
#include <stdint.h>

namespace {
constexpr int NBATCH = 4, HQ = 32, HKV = 8, SKV = 8192, DH = 128;
constexpr int NBH = NBATCH * HKV;
constexpr int NSPLIT = 8;
constexpr int NCW = 4;
constexpr int NT = 16;
constexpr int NLT = 2 * NT - 1;
constexpr int NTHR = (NCW + 1) * 64;
constexpr int KV_BYTES = NBH * SKV * DH * 4;
constexpr int NFL = 22;

constexpr int SB_[9] = {13, 14, 11, 12, 15, 16, 17, 10, 19};
constexpr int BB_[8] = {23, 22, 25, 18, 20, 21, 24, 26};
__host__ __device__ constexpr int pos2off(int x) { int o = 0; for (int k = 0; k < 9; ++k) o |= ((x >> k) & 1) << SB_[k]; return o; }
__host__ __device__ constexpr int pos2g(int x) { return ((pos2off(x) >> 15) & 7) | ((pos2off(x) >> 16) & 8); }
__host__ __device__ constexpr int pos2vp(int x) { return pos2g(x) * 128 + ((pos2off(x) >> 8) & 0x7c); }
static_assert(pos2off(511) == 0xbfc00, "a workgroup covers 16 whole groups");
static_assert(pos2g(15) == 0, "four consecutive rows of a consumer lie in one group");
static_assert(BB_[3] < 22 && BB_[4] < 22 && BB_[5] < 22, "blockIdx bits 3-5 pick the key split");
static_assert(SB_[7] == 10 && SB_[8] == 19, "the flipped position bits are re-derived from address bits 10 and 19");

typedef unsigned u32x4 __attribute__((ext_vector_type(4)));
typedef unsigned u32x2 __attribute__((ext_vector_type(2)));
typedef int i32x4 __attribute__((ext_vector_type(4)));
typedef float f32x4 __attribute__((ext_vector_type(4)));
typedef float f32x2 __attribute__((ext_vector_type(2)));
typedef __bf16 bf16x2 __attribute__((ext_vector_type(2)));
typedef __bf16 bf16x8 __attribute__((ext_vector_type(8)));
template <bool B> struct BoolC { static constexpr bool value = B; };
#define LAS __attribute__((address_space(3)))

__device__ __forceinline__ unsigned pk(float lo, float hi) {
  bf16x2 v = {(__bf16)lo, (__bf16)hi};
  return __builtin_bit_cast(unsigned, v);
}
__device__ __forceinline__ float lo16(unsigned u) { return __builtin_bit_cast(float, u << 16); }
__device__ __forceinline__ float hi16(unsigned u) { return __builtin_bit_cast(float, u & 0xffff0000u); }
__device__ __forceinline__ float rbf(float x) { return (float)(__bf16)x; }
__device__ __forceinline__ f32x4 mfma(u32x4 a, u32x4 b, f32x4 c) {
  return __builtin_amdgcn_mfma_f32_16x16x32_bf16(__builtin_bit_cast(bf16x8, a), __builtin_bit_cast(bf16x8, b), c, 0, 0, 0);
}
template <int CTRL> __device__ __forceinline__ float dpp(float x) {
  return __builtin_bit_cast(float, __builtin_amdgcn_mov_dpp(__builtin_bit_cast(int, x), CTRL, 0xf, 0xf, true));
}
__device__ __forceinline__ float wave_sum4(const float (&x)[4]) {
  auto s = __builtin_amdgcn_permlane32_swap(__float_as_uint(x[0]), __float_as_uint(x[2]), false, false);
  auto t = __builtin_amdgcn_permlane32_swap(__float_as_uint(x[1]), __float_as_uint(x[3]), false, false);
  auto u = __builtin_amdgcn_permlane16_swap(__float_as_uint(__uint_as_float(s[0]) + __uint_as_float(s[1])),
                                            __float_as_uint(__uint_as_float(t[0]) + __uint_as_float(t[1])), false, false);
  float y = __uint_as_float(u[0]) + __uint_as_float(u[1]);
  y += dpp<0x128>(y); y += dpp<0x124>(y); y += dpp<0x122>(y); y += dpp<0x121>(y);
  return y;
}
#define WAVE_FENCE() do { __builtin_amdgcn_fence(__ATOMIC_ACQ_REL, "wavefront"); __builtin_amdgcn_wave_barrier(); } while (0)
#define SB() __builtin_amdgcn_sched_barrier(0)
#define LDS_WAIT() asm volatile("s_waitcnt lgkmcnt(0)" ::: "memory")
#define LDS_BARRIER() asm volatile("s_waitcnt lgkmcnt(0)\n\ts_barrier" ::: "memory")

constexpr int SLOT = 32 * 1024, RING = 3 * SLOT;
constexpr int KZ_OFF = RING, KS_OFF = KZ_OFF + 4096;
constexpr int VS_OFF = KS_OFF + 4096;
constexpr int VZ_OFF = VS_OFF + 2048;
constexpr int Q_OFF = VZ_OFF + 2048;
constexpr int OG_OFF = Q_OFF + 1024;
constexpr int PRIV = OG_OFF + 2048;
constexpr int KT_RS = 144, KT_HALF = 16 * KT_RS, KT_BYTES = 2 * KT_HALF;
constexpr int PT_OFF = KT_BYTES;
constexpr int SC_OFF = PT_OFF + 1024;
constexpr int PRIV_W = SC_OFF + 4096;
constexpr int CTL = PRIV + NCW * PRIV_W;
constexpr int C_FREE = CTL, C_FULL = CTL + 32, C_MX = CTL + 48, C_LC = C_MX + NCW * 16, C_CZ = C_LC + NCW * 16, C_TG = C_CZ + NCW * 16;
constexpr int LDS_BYTES = C_TG + 16;
static_assert(LDS_BYTES <= 160 * 1024, "LDS");

constexpr int X_ECHO = 128, X_REC = 256, X_SLOT = 1024, XREC = X_SLOT + NSPLIT * 2048;
constexpr int OOB = 0x40000000, SC1 = 16;
constexpr unsigned SPIN_CAP = 1u << 21;

__global__ void __launch_bounds__(NTHR, 2) __attribute__((aligned(8192)))
kv_decode(const int* __restrict__ qk, const int* __restrict__ qv,
          const unsigned short* __restrict__ kzr, const unsigned short* __restrict__ ksc,
          const unsigned short* __restrict__ q,
          const unsigned short* __restrict__ vsc, const unsigned short* __restrict__ vzr,
          unsigned char* ws, unsigned* out) {
  __shared__ __attribute__((aligned(16))) unsigned char smem[LDS_BYTES];
  const int tid = threadIdx.x, lane = tid & 63;
  const int wv = __builtin_amdgcn_readfirstlane(tid >> 6);
  const int bid = blockIdx.x;
  int gbase = 0;
#pragma unroll
  for (int k = 0; k < 8; ++k) {
    if (k > 0 && BB_[k] == BB_[k - 1] + 1) continue;
    const int n = (k < 7 && BB_[k + 1] == BB_[k] + 1) ? 3 : 1;
    gbase |= ((bid >> k) & n) << BB_[k];
  }
  const int voff = lane * 16;
  asm volatile("" : "+s"(gbase) : "s"(qk), "s"(qv), "s"(kzr), "s"(ksc), "s"(q), "s"(vsc), "s"(vzr), "s"(ws), "s"(out));
  const int split = (bid >> 3) & 7, bh = gbase >> 22;
  const int gb = gbase | (((bid >> 4) & 1) << SB_[7]) | (((bid >> 3) & 1) << SB_[8]);
  if (wv == 0) {
    __builtin_amdgcn_s_setprio(3);
    const unsigned lb = (unsigned)(size_t)(LAS unsigned char*)smem;
    const unsigned fa = lb + C_FULL, fr = lb + C_FREE;
    const int k7 = voff + ((unsigned)gbase >> 7), k8 = voff + ((unsigned)gbase >> 8);
#define PAR2(M0, VA, P, O0, O1, POL) asm volatile("s_mov_b32 m0, %0\n\ts_nop 0\n\tglobal_load_lds_dwordx4 %1, %2 offset:" #O0 POL "\n\tglobal_load_lds_dwordx4 %1, %2 offset:" #O1 POL \
                                               :: "s"(lb + (M0)), "v"(VA), "s"(P) : "memory")
#define PAR1(M0, VA, P, POL) asm volatile("s_mov_b32 m0, %0\n\ts_nop 0\n\tglobal_load_lds_dwordx4 %1, %2" POL :: "s"(lb + (M0)), "v"(VA), "s"(P) : "memory")
    PAR2(KZ_OFF, k7, kzr, 0, 1024, " nt"); PAR2(KS_OFF, k7, ksc, 0, 1024, " nt");
    PAR1(Q_OFF, voff + bh * 1024, q, "");
    PAR2(KZ_OFF, k7 + 2048, kzr, 2048, 3072, " nt"); PAR2(KS_OFF, k7 + 2048, ksc, 2048, 3072, " nt");
    PAR1(VS_OFF, k8, vsc, " nt"); PAR1(VZ_OFF, k8, vzr, " nt"); PAR1(VS_OFF + 1024, k8 + 2048, vsc, " nt"); PAR1(VZ_OFF + 1024, k8 + 2048, vzr, " nt");
    PAR2(OG_OFF, voff + bh * XREC + X_SLOT + split * 2048, ws, 0, 1024, " sc1");
    static_assert(pos2off(127) == 0x3f800 && SB_[2] < SB_[3] && SB_[3] < SB_[4] && SB_[4] < SB_[5] && SB_[5] < SB_[6], "address walk of the loader");
    const int vo1 = voff + pos2off(1) - 1024, vo2 = voff + pos2off(2) - 2048, vo3 = voff + pos2off(3) - 3072;
    unsigned mb = lb;
    int so = gb, it = 0;
    int cnt = (4 - NFL) & ~3;
    unsigned long long src = (unsigned long long)qk;
    i32x4 f;
    asm volatile("" : "=v"(f));
#define ROW(J, V) "s_waitcnt vmcnt(%9)\n\tbuffer_load_dwordx4 " V ", %6, %7 offen offset:" #J "*1024 nt lds\n\t"
#pragma unroll 1
    do {
      const __amdgpu_buffer_rsrc_t rs = __builtin_amdgcn_make_buffer_rsrc((void*)src, 0, KV_BYTES, 0x00020000);
      asm volatile("s_mov_b32 m0, %1\n\t" ROW(0, "%2") ROW(1, "%3") ROW(2, "%4") ROW(3, "%5") "ds_write_b32 %8, %0\n\tv_add_u32 %0, 4, %0\n\ts_nop 4"
                   : "+v"(cnt) : "s"(mb), "v"(voff), "v"(vo1), "v"(vo2), "v"(vo3), "s"(rs), "s"(so), "v"(fa), "i"(NFL - 1) : "memory");
      ++it;
      mb += 4096;
      so = ((so | pos2off(3)) + pos2off(4)) & ~pos2off(3);
      if ((it & 7) == 7) asm volatile("ds_read_b128 %0, %1" : "=v"(f) : "v"(fr) : "memory");
      if ((it & 7) == 0) {
        const int T = it >> 3;
        if (T == 1) LDS_BARRIER();
        if (mb == lb + RING) mb = lb;
        if ((T & 3) == 0) { so = gb ^ pos2off((T & 12) << 5); if (T == NT) src = (unsigned long long)qv; }
        asm volatile("s_waitcnt lgkmcnt(1)" : "+v"(f) :: "memory");
        while (T > 2 && T < NLT && __builtin_amdgcn_readfirstlane(min(min(f[0], f[1]), min(f[2], f[3]))) < T - 2) {
          __builtin_amdgcn_s_sleep(1);
          asm volatile("ds_read_b128 %0, %1\n\ts_waitcnt lgkmcnt(0)" : "=v"(f) : "v"(fr) : "memory");
        }
      }
    } while (it < NLT * 8);
    static_assert(((4 - NFL) & ~3) == -20, "the drain starts 20 rows before the end");
#define DRAIN(N) "s_waitcnt vmcnt(" #N ")\n\tds_write_b32 %1, %0\n\tv_add_u32 %0, 4, %0\n\t"
    asm volatile(DRAIN(20) DRAIN(16) DRAIN(12) DRAIN(8) DRAIN(4) DRAIN(0) "s_waitcnt lgkmcnt(0)\n\ts_endpgm" : "+v"(cnt) : "v"(fa) : "memory");
  }
  const int w = wv - 1;
  const int lg = lane >> 4, c = lane & 15, r = lane & 31, h = lane >> 5;
  volatile LAS int* ctl = (volatile LAS int*)(smem + CTL);
  if (lane == 0) { ctl[w] = 0; ctl[4 + w] = 0; }
#define POST_STAGE(v) do { LDS_WAIT(); if (lane == 0) ctl[4 + w] = (v); } while (0)
#define WAIT_STAGE(v) do { while (__builtin_amdgcn_ballot_w64(ctl[4 + (lane & 3)] < (v)) != 0) __builtin_amdgcn_s_sleep(1); asm volatile("" ::: "memory"); } while (0)
#define WAIT_ROWS(n) do { while (__builtin_amdgcn_readfirstlane(ctl[8]) < (n)) __builtin_amdgcn_s_sleep(1); asm volatile("" ::: "memory"); } while (0)
  const __amdgpu_buffer_rsrc_t rsX = __builtin_amdgcn_make_buffer_rsrc((void*)(ws + bh * XREC), 0, XREC, 0x00020000);
  const int xo = X_SLOT + w * 512 + lane * 8;
  unsigned oldc = 0;
  if (tid < 64 + 6) asm volatile("buffer_load_dword %0, %1, %2, 0 offen sc1" : "+v"(oldc) : "v"(lane * 128), "s"(rsX) : "memory");
  SB();
  LDS_BARRIER();
  unsigned char* kt = smem + PRIV + w * PRIV_W;
  const unsigned char* ringw = smem + w * 1024 + voff;
  unsigned char* kw = kt + (r >> 4) * KT_HALF + h * KT_RS + (r & 15) * 8;
  const unsigned char* kr = kt + (lg & 1) * KT_HALF + c * KT_RS + (lg >> 1) * 16;
  WAIT_ROWS(4);
  const u32x2 oldg = *(const u32x2*)(smem + OG_OFF + w * 512 + lane * 8);
  unsigned tag = (__builtin_amdgcn_readfirstlane(oldg[0]) + 1u) & 255u;
  while (__any(((oldg[0] & 255u) == tag) | ((oldg[1] & 255u) == tag))) tag = (tag + 1u) & 255u;
  asm volatile("s_waitcnt vmcnt(0)" : "+v"(oldc) :: "memory");
  const unsigned c0 = __builtin_amdgcn_readfirstlane(oldc);
  const u32x2 echo = {c0 ^ 0x9e3779b9u, c0 + 0x7f4a7c15u};
  if (lane == 0) {
    *(unsigned*)(smem + C_TG + w * 4) = tag;
    if (w == 0) __builtin_amdgcn_raw_buffer_store_b64(echo, rsX, X_ECHO, split * 8, SC1);
  }
  u32x4 Q[4];
#pragma unroll
  for (int s = 0; s < 4; ++s) Q[s] = *(const u32x4*)(smem + Q_OFF + (c >> 2) * (DH * 2) + 128 * (lg & 1) + 16 * (lg >> 1) + 32 * s);
  constexpr float SCALE = 0.088388347648318440f, LOG2E = 1.4426950408889634f;
  float mx = -INFINITY;
  int so = 0;

#pragma unroll 1
  for (int H = 0; H < 2 * NT; ++H) {
    const unsigned char* kp = smem + KZ_OFF + ((pos2g(H << 4) * 256) ^ ((gb >> 8) & 2048)) + r * 8;
    const u32x2 kzp = *(const u32x2*)kp, ksp = *(const u32x2*)(kp + 4096);
    WAIT_ROWS(H * 16 + 16);
    u32x4 x[4];
#pragma unroll
    for (int i = 0; i < 4; ++i) x[i] = *(const u32x4*)(ringw + so + i * 4096);
    LDS_WAIT();
    if (lane == 0) ctl[w] = (H + 1) >> 1;
    asm volatile("" : "+v"(x[0]), "+v"(x[1]), "+v"(x[2]), "+v"(x[3]) :: "memory");
    so = so == RING - SLOT / 2 ? 0 : so + SLOT / 2;
    const float kz0 = lo16(kzp[0]), kz1 = hi16(kzp[0]), kz2 = lo16(kzp[1]), kz3 = hi16(kzp[1]);
    const float ks0 = lo16(ksp[0]), ks1 = hi16(ksp[0]), ks2 = lo16(ksp[1]), ks3 = hi16(ksp[1]);
    unsigned char* kwh = kw + (H & 1) * 8 * KT_RS;
#pragma unroll
    for (int i = 0; i < 4; ++i) {
      const unsigned t01 = pk((float)(int)x[i][0] - kz0, (float)(int)x[i][1] - kz1), t23 = pk((float)(int)x[i][2] - kz2, (float)(int)x[i][3] - kz3);
      u32x2 kk;
      kk[0] = pk(lo16(t01) * ks0, hi16(t01) * ks1);
      kk[1] = pk(lo16(t23) * ks2, hi16(t23) * ks3);
      *(u32x2*)(kwh + i * 2 * KT_RS) = kk;
    }
    if (H & 1) {
      WAVE_FENCE();
      f32x4 d = {0.f, 0.f, 0.f, 0.f};
#pragma unroll
      for (int s = 0; s < 4; ++s) {
        const u32x4 bf = *(const u32x4*)(kr + s * 32);
        d = mfma(Q[s], bf, d);
      }
      WAVE_FENCE();
      const float sc = rbf(rbf(d[0]) * SCALE);
      mx = fmaxf(mx, sc);
      *(float*)(kt + SC_OFF + (H >> 1) * 256 + c * 16 + lg * 4) = sc;
    }
  }
  mx = fmaxf(mx, dpp<0x128>(mx)); mx = fmaxf(mx, dpp<0x124>(mx)); mx = fmaxf(mx, dpp<0x122>(mx)); mx = fmaxf(mx, dpp<0x121>(mx));
  if (c == 0) *(float*)(smem + C_MX + w * 16 + lg * 4) = mx;
  POST_STAGE(1);
  WAIT_STAGE(1);
  f32x4 m = *(const f32x4*)(smem + C_MX);
#pragma unroll
  for (int ww = 1; ww < NCW; ++ww) {
    const f32x4 o = *(const f32x4*)(smem + C_MX + ww * 16);
#pragma unroll
    for (int j = 0; j < 4; ++j) m[j] = fmaxf(m[j], o[j]);
  }
  float lsum[4], cz[4];
  f32x4 acc[4];
#pragma unroll
  for (int j = 0; j < 4; ++j) { lsum[j] = 0.f; cz[j] = 0.f; acc[j] = f32x4{0.f, 0.f, 0.f, 0.f}; }
  const bool live = ((lane >> 3) & 1) == ((lane >> 4) & 1);
  const int lanev = pos2vp((lg << 5) | ((c >> 1) << 2) | w) + (c & 1) * 2 + ((gb >> 8) & 4) + ((gb >> 9) & 1024);
  unsigned char* pt = kt + PT_OFF;
  const unsigned fulla = (unsigned)(size_t)(LAS unsigned char*)smem + C_FULL, ringa = (unsigned)(size_t)(LAS unsigned char*)smem + w * 1024 + voff;
  const __amdgpu_buffer_rsrc_t rsV = __builtin_amdgcn_make_buffer_rsrc((void*)qv, 0, KV_BYTES, 0x00020000);
  const int lastb = gb ^ pos2off((NT - 1) << 5);
  const int lastv = pos2off(lg) + w * 128 + c * 8;
  u32x2 dr[16];
  u32x2 ev = __builtin_amdgcn_raw_buffer_load_b64(rsX, X_ECHO + (lane & 7) * 8, w == 0 ? 0 : OOB, SC1);
  unsigned tk = 0;
  auto v_tile = [&](const int T, auto peel_) {
    constexpr bool PEEL = decltype(peel_)::value;
    u32x4 A = *(const u32x4*)(pt + (T & 3) * 256 + h * 128 + (lane & 7) * 16);
    if (!live) A = u32x4{0u, 0u, 0u, 0u};
    u32x4 Bf[4];
#pragma unroll
    for (int mm = 0; mm < 4; ++mm) {
      u32x4 xa, xb;
      while (true) {
        int fv;
        asm volatile("ds_read_b32 %0, %3\n\tds_read_b128 %1, %4 offset:%5\n\tds_read_b128 %2, %4 offset:%6\n\ts_waitcnt lgkmcnt(0)"
                     : "=&v"(fv), "=&v"(xa), "=&v"(xb) : "v"(fulla), "v"(ringa + so), "i"(mm * 8192), "i"(mm * 8192 + 4096) : "memory");
        if (__builtin_amdgcn_readfirstlane(fv) >= (NT + T) * 32 + mm * 8 + 8) break;
        if (!PEEL) __builtin_amdgcn_s_sleep(2);
      }
      if (PEEL && (mm & 1) == 0) {
#pragma unroll
        for (int i = 4 * mm; i < 4 * mm + 8; ++i) { dr[i] = __builtin_amdgcn_raw_buffer_load_b64(rsV, lastv + (i & 1) * 512, lastb + pos2off((i >> 1) << 2), 2); SB(); }
      }
      if (mm == 3 && lane == 0) ctl[w] = NT + T + 1;
#pragma unroll
      for (int e = 0; e < 4; ++e) { unsigned t = pk((float)(int)xa[e], (float)(int)xb[e]); asm volatile("" : "+v"(t)); Bf[e][mm] = t; }
    }
    so = so == 2 * SLOT ? 0 : so + SLOT;
#pragma unroll
    for (int e = 0; e < 4; ++e) acc[e] = mfma(A, Bf[e], acc[e]);
    WAVE_FENCE();
  };
#pragma unroll 1
  for (int T = 0; T < NT - 2; ++T) {
    if ((T & 3) == 0) {
      const int tk2 = lanev ^ pos2vp(T << 5);
      const f32x4 S = *(const f32x4*)(kt + SC_OFF + T * 256 + voff);
      const float vs = lo16(*(const unsigned short*)(smem + VS_OFF + tk2));
      const float vz = lo16(*(const unsigned short*)(smem + VZ_OFF + tk2));
      unsigned char* pw = pt + lg * 256 + (c & 1) * 128 + (c >> 1) * 2;
#pragma unroll
      for (int j = 0; j < 4; ++j) {
        const float p = __builtin_amdgcn_exp2f((S[j] - m[j]) * LOG2E);
        const float pv = p * vs;
        const float hi = rbf(pv), lo = rbf(pv - hi);
        lsum[j] += p;
        cz[j] += (hi + lo) * vz;
        const int rt = (j & 1) + 4 * (j >> 1);
        *(unsigned short*)(pw + rt * 16) = (unsigned short)(__builtin_bit_cast(unsigned, hi) >> 16);
        *(unsigned short*)(pw + (rt + 2) * 16) = (unsigned short)(__builtin_bit_cast(unsigned, lo) >> 16);
      }
      if (T == NT - 4) {
        const float lv = wave_sum4(lsum), cv = wave_sum4(cz);
        if (c == 0) { *(float*)(smem + C_LC + w * 16 + lg * 4) = lv; *(float*)(smem + C_CZ + w * 16 + lg * 4) = cv; }
        POST_STAGE(2);
        if (w == 0) {
          WAIT_STAGE(2);
          if (lane < 4) {
            float L = 0.f;
#pragma unroll
            for (int ww = 0; ww < NCW; ++ww) L += *(const float*)(smem + C_LC + ww * 16 + lane * 4);
            const float Ma = (lane & 1) ? m[1] : m[0], Mb = (lane & 1) ? m[3] : m[2], M = (lane & 2) ? Mb : Ma;
            __builtin_amdgcn_raw_buffer_store_b128(u32x4{__float_as_uint(M), __float_as_uint(L), *(const unsigned*)(smem + C_TG + lane * 4), 0u},
                                                   rsX, X_REC + lane * 16, split * 64, SC1);
          }
        }
      }
      WAVE_FENCE();
    }
    v_tile(T, BoolC<false>{});
    if (T == NT - 3 && w == 0) {
#pragma unroll 1
      for (unsigned spin = 0; spin < SPIN_CAP && !__all((ev[0] == echo[0]) & (ev[1] == echo[1])); ++spin) {
        __builtin_amdgcn_s_sleep(8);
        asm volatile("" ::: "memory");
        ev = __builtin_amdgcn_raw_buffer_load_b64(rsX, X_ECHO + (lane & 7) * 8, 0, SC1);
      }
      asm volatile("s_waitcnt vmcnt(0)" ::: "memory");
      tk = 1u;
      if (lane == 0) asm volatile("buffer_atomic_add %0, off, %1, 0 sc0" : "+v"(tk) : "s"(rsX) : "memory");
    }
  }
  v_tile(NT - 2, BoolC<true>{});
  SB();
  {
    f32x4 o0, o1;
#pragma unroll
    for (int e = 0; e < 4; ++e) { o0[e] = acc[e][0] + acc[e][2]; o1[e] = acc[e][1] + acc[e][3]; }
    float* ow = (float*)kt + 2 * (lg & 1) * DH + 64 * (lg >> 1) + 4 * c;
    *(f32x4*)ow = o0;
    *(f32x4*)(ow + DH) = o1;
  }
  if (w == 0) {
    asm volatile("s_waitcnt vmcnt(16)" : "+v"(tk) :: "memory");
    if (lane == 0) ctl[9] = tk - c0;
  }
  LDS_BARRIER();
  const bool last = __builtin_amdgcn_readfirstlane(ctl[9]) == NSPLIT - 1;
  if (last && tid < 64 + 8) __builtin_amdgcn_raw_buffer_store_b64(u32x2{0u, 0u}, rsX, X_ECHO + lane * 8, 0, SC1);
  const u32x2 mlv = __builtin_amdgcn_raw_buffer_load_b64(rsX, X_REC + lg * 16 + (c & 7) * 64, last ? 0 : OOB, SC1);
  const unsigned tgv = __builtin_amdgcn_raw_buffer_load_b32(rsX, X_REC + 8 + w * 16 + (c & 7) * 64, last ? 0 : OOB, SC1);
  SB();
  f32x2 O = {0.f, 0.f};
#pragma unroll
  for (int ww = 0; ww < NCW; ++ww)
    O += *(const f32x2*)(smem + PRIV + ww * PRIV_W + (lg * DH + 32 * w + 2 * c) * 4) - *(const float*)(smem + C_CZ + ww * 16 + lg * 4);
  const unsigned char* pa = smem + PRIV + lg * PRIV_W + PT_OFF + 3 * 256 + (((c >> 2) & 1) + 2 * (c & 1) + 4 * (c >> 3)) * 16;
  const u32x4 A0 = *(const u32x4*)pa, A1 = *(const u32x4*)(pa + 128);
  f32x4 d0 = {O[0], 0.f, 0.f, 0.f}, d1 = {O[1], 0.f, 0.f, 0.f};
  int ofs[NSPLIT];
  u32x2 pv[NSPLIT];
#pragma unroll
  for (int k = 1; k < NSPLIT; ++k) ofs[k] = last ? (k ^ split) * 2048 : OOB;
  SB();
  unsigned oo = ((bh * 4 + lg) * 64 + 16 * w + c) * 4;
  asm volatile("" : "+v"(d0), "+v"(d1), "+v"(oo));
  SB();
  u32x4 B[2][2];
#pragma unroll
  for (int mm = 0; mm < 4; ++mm) {
#pragma unroll
    for (int ks = 0; ks < 2; ++ks)
#pragma unroll
      for (int e = 0; e < 2; ++e) B[ks][e][mm] = pk((float)(int)dr[4 * mm + ks][e], (float)(int)dr[4 * mm + 2 + ks][e]);
    SB();
    if (mm == 1) {
#pragma unroll
      for (int k = 1; k < NSPLIT; ++k) pv[k] = __builtin_amdgcn_raw_buffer_load_b64(rsX, xo, ofs[k], SC1);
      SB();
    }
  }
  d0 = mfma(A0, B[0][0], d0); d1 = mfma(A0, B[0][1], d1);
  d0 = mfma(A1, B[1][0], d0); d1 = mfma(A1, B[1][1], d1);
  pv[0] = u32x2{__float_as_uint(d0[0] + d0[1]) & ~255u, __float_as_uint(d1[0] + d1[1]) & ~255u};
  if (!last) {
    __builtin_amdgcn_raw_buffer_store_b64(u32x2{pv[0][0] | tag, pv[0][1] | tag}, rsX, xo, split * 2048, SC1);
    asm volatile("s_endpgm" ::: "memory");
  }
  SB();
  float wk[NSPLIT];
  unsigned tg[NSPLIT];
  {
    float M = __uint_as_float(mlv[0]);
    M = fmaxf(M, dpp<0xB1>(M)); M = fmaxf(M, dpp<0x4E>(M)); M = fmaxf(M, dpp<0x141>(M));
    float wt = __builtin_amdgcn_exp2f((__uint_as_float(mlv[0]) - M) * LOG2E);
    float L = __fmul_rn(wt, __uint_as_float(mlv[1]));
    L += dpp<0xB1>(L); L += dpp<0x4E>(L); L += dpp<0x141>(L);
    wt *= __builtin_amdgcn_rcpf(L);
#pragma unroll
    for (int k = 0; k < NSPLIT; ++k) {
      wk[k] = __shfl(wt, (lane & 48) | (k ^ split), 64);
      tg[k] = __builtin_amdgcn_readlane(tgv, k ^ split);
      asm volatile("" : "+v"(wk[k]), "+s"(tg[k]));
    }
  }
  u32x2 nx[NSPLIT];
#pragma unroll 1
  for (unsigned spin = 0; ; ++spin) {
#pragma unroll
    for (int k = 1; k < NSPLIT; ++k) nx[k] = __builtin_amdgcn_raw_buffer_load_b64(rsX, xo, ofs[k], SC1);
    SB();
    unsigned bad = 0;
#pragma unroll
    for (int k = 1; k < NSPLIT; ++k) { pv[k][0] ^= tg[k]; pv[k][1] ^= tg[k]; bad |= pv[k][0] | pv[k][1]; }
    if (__builtin_amdgcn_ballot_w64((bad & 255u) != 0) == 0 || spin == SPIN_CAP) break;
    asm volatile("" ::: "memory");
#pragma unroll
    for (int k = 1; k < NSPLIT; ++k) pv[k] = nx[k];
  }
  {
#pragma clang fp contract(off)
    f32x2 t[NSPLIT];
#pragma unroll
    for (int k = 0; k < NSPLIT; ++k) { const u32x2 xk = pv[k]; t[k] = __builtin_bit_cast(f32x2, xk) * wk[k]; }
    const f32x2 rr = ((t[0] + t[1]) + (t[2] + t[3])) + ((t[4] + t[5]) + (t[6] + t[7]));
    *(unsigned*)((char*)out + oo) = pk(rr[0], rr[1]);
  }
}
}

extern "C" void kernel_launch(void* const* d_in, const int* in_sizes, int n_in, void* d_out, int out_size,
                              void* d_ws, size_t ws_size, hipStream_t stream) {
  const unsigned short* q = (const unsigned short*)d_in[0];
  const int* qk = (const int*)d_in[1];
  const int* qv = (const int*)d_in[2];
  const unsigned short* ks = (const unsigned short*)d_in[3];
  const unsigned short* kz = (const unsigned short*)d_in[4];
  const unsigned short* vs = (const unsigned short*)d_in[5];
  const unsigned short* vz = (const unsigned short*)d_in[6];
  kv_decode<<<NBH * NSPLIT, NTHR, 0, stream>>>(qk, qv, kz, ks, q, vs, vz, (unsigned char*)d_ws, (unsigned*)d_out);
}
```
